```python
import torch

def _softmax(x, axis=-1):
    return torch.softmax(x, dim=axis)
def _gelu(x):
    return torch.nn.functional.gelu(x, approximate="tanh")
def _silu(x):
    return torch.nn.functional.silu(x)
def _sigmoid(x):
    return torch.sigmoid(x)
def _layernorm(x, axis=-1, eps=1e-5):
    m = x.mean(dim=axis, keepdim=True)
    v = x.var(dim=axis, keepdim=True, unbiased=False)
    return (x - m) / torch.sqrt(v + eps)
def _scatter_add(tgt, idx, src, axis=0):
    return tgt.clone().index_add_(axis, idx, src)
def _segment_sum(src, seg, n):
    out = torch.zeros((n,) + src.shape[1:], dtype=src.dtype)
    return out.index_add_(0, seg, src)
def _pad(x, pw):
    flat = tuple(v for pair in reversed(pw) for v in pair)
    return torch.nn.functional.pad(x, flat)

kernel_name = 'concat_heads__mha_block_cached__gqa_block_cached__near_ridge'

def setup_inputs():

    _w = (lambda a: (lambda t: t.to(torch.bfloat16) if t.is_floating_point() else t)(torch.as_tensor(a)))
    def _A_setup():
        B, Sq, D = 8, 144, 448
        H, Sk, Dh = 16, 512, 256
        return {
            "x": _w((lambda *s: torch.randn(*s, dtype=torch.bfloat16) * 0.8)(B, Sq, D)),
            "k_cache": _w((lambda *s: torch.randn(*s, dtype=torch.bfloat16) * 0.8)(B, H, Sk, Dh)),
            "v_cache": _w((lambda *s: torch.randn(*s, dtype=torch.bfloat16) * 0.05)(B, H, Sk, Dh)),
            "wq": _w((lambda *s: torch.randn(*s, dtype=torch.bfloat16) * 0.8)(D, H * Dh) * (D ** -0.5)),
            "wo": _w((lambda *s: torch.randn(*s, dtype=torch.bfloat16) * 0.05)(H * Dh, D) * ((H * Dh) ** -0.5)),
        }
    _A_in = _A_setup()

    def _B_setup():
        B, Sq, D = 8, 144, 448
        Hq, Hkv, Sk, Dh = 16, 16, 512, 256
        assert Hq % Hkv == 0, (Hq, Hkv)
        return {
            "x": _w((lambda *s: torch.randn(*s, dtype=torch.bfloat16) * 0.8)(B, Sq, D)),
            "k_cache": _w((lambda *s: torch.randn(*s, dtype=torch.bfloat16) * 0.8)(B, Hkv, Sk, Dh)),
            "v_cache": _w((lambda *s: torch.randn(*s, dtype=torch.bfloat16) * 0.05)(B, Hkv, Sk, Dh)),
            "wq": _w((lambda *s: torch.randn(*s, dtype=torch.bfloat16) * 0.8)(D, Hq * Dh) * (D ** -0.5)),
            "wo": _w((lambda *s: torch.randn(*s, dtype=torch.bfloat16) * 0.05)(Hq * Dh, D) * ((Hq * Dh) ** -0.5)),
        }
    _B_in = _B_setup()

    return {
        "A_x": _A_in["x"],
        "A_k_cache": _A_in["k_cache"],
        "A_v_cache": _A_in["v_cache"],
        "A_wq": _A_in["wq"],
        "A_wo": _A_in["wo"],
        "B_x": _B_in["x"],
        "B_k_cache": _B_in["k_cache"],
        "B_v_cache": _B_in["v_cache"],
        "B_wq": _B_in["wq"],
        "B_wo": _B_in["wo"],
    }

def reference(A_x, A_k_cache, A_v_cache, A_wq, A_wo, B_x, B_k_cache, B_v_cache, B_wq, B_wo):
    def _A_ref(x, k_cache, v_cache, wq, wo):
        B, Sq, D = x.shape
        H, Dh = 16, 256
        q = (lambda x, *d: x.permute(*d))((x @ wq).reshape(B, Sq, H, Dh), 0, 2, 1, 3)
        scores = torch.einsum("bhqd,bhkd->bhqk", q, k_cache) * (Dh ** -0.5)
        p = _softmax(scores, axis=-1)
        o = torch.einsum("bhqk,bhkd->bhqd", p, v_cache)
        return (lambda x, *d: x.permute(*d))(o, 0, 2, 1, 3).reshape(B, Sq, H * Dh) @ wo

    def _B_ref(x, k_cache, v_cache, wq, wo):
        B, Sq, D = x.shape
        Hq, Dh = 16, 256
        Hkv = k_cache.shape[1]
        rep = Hq // Hkv
        q = (lambda x, *d: x.permute(*d))((x @ wq).reshape(B, Sq, Hq, Dh), 0, 2, 1, 3)
        k = (lambda x, n, axis: torch.repeat_interleave(x, n, dim=axis))(k_cache, rep, axis=1)
        v = (lambda x, n, axis: torch.repeat_interleave(x, n, dim=axis))(v_cache, rep, axis=1)
        scores = torch.einsum("bhqd,bhkd->bhqk", q, k) * (Dh ** -0.5)
        p = _softmax(scores, axis=-1)
        o = torch.einsum("bhqk,bhkd->bhqd", p, v)
        return (lambda x, *d: x.permute(*d))(o, 0, 2, 1, 3).reshape(B, Sq, Hq * Dh) @ wo

    a = _A_ref(A_x, A_k_cache, A_v_cache, A_wq, A_wo)
    b = _B_ref(B_x, B_k_cache, B_v_cache, B_wq, B_wo)
    return torch.concatenate([a, b], axis=-1)
```

```cpp
#include <hip/hip_runtime.h>
#include <hip/hip_bf16.h>
#include <stdint.h>

typedef short bf16x8 __attribute__((ext_vector_type(8)));
typedef short bf16x4 __attribute__((ext_vector_type(4)));
typedef float f32x16 __attribute__((ext_vector_type(16)));
typedef float f32x4 __attribute__((ext_vector_type(4)));
typedef unsigned u32x4 __attribute__((ext_vector_type(4)));
typedef unsigned u32x2 __attribute__((ext_vector_type(2)));
typedef __bf16 bf2_t __attribute__((ext_vector_type(2)));
typedef __attribute__((address_space(3))) bf16x4* lds_b4_ptr;

#define DI __device__ __forceinline__

constexpr int NB = 8, SQ = 144, DM = 448, NH = 16, SK = 512, DH = 256;
constexpr int SLOT = 32768;
constexpr int LDS_BYTES = 5 * SLOT;
constexpr size_t OF_PER_P = (size_t)36 * 256 * 512;

DI unsigned pack2(float a, float b) {
  bf2_t v = {(__bf16)a, (__bf16)b};
  return __builtin_bit_cast(unsigned, v);
}
DI bf16x8 pack8(float a0, float a1, float a2, float a3, float a4, float a5, float a6, float a7) {
  u32x4 w = {pack2(a0, a1), pack2(a2, a3), pack2(a4, a5), pack2(a6, a7)};
  return __builtin_bit_cast(bf16x8, w);
}
DI f32x16 mfma32(bf16x8 a, bf16x8 b, f32x16 c) { return __builtin_amdgcn_mfma_f32_32x32x16_bf16(a, b, c, 0, 0, 0); }
DI f32x4 mfma16x32(bf16x8 a, bf16x8 b, f32x4 c) { return __builtin_amdgcn_mfma_f32_16x16x32_bf16(a, b, c, 0, 0, 0); }
DI f32x4 mfma16x16(bf16x4 a, bf16x4 b, f32x4 c) { return __builtin_amdgcn_mfma_f32_16x16x16bf16_1k(a, b, c, 0, 0, 0); }

#define WAITV(n) asm volatile("s_waitcnt vmcnt(%0)" ::"n"(n) : "memory")
#define BARRIER() do { asm volatile("s_waitcnt lgkmcnt(0)" ::: "memory"); __builtin_amdgcn_s_barrier(); asm volatile("" ::: "memory"); } while (0)

DI bf16x4 tr_read(const char* smem, unsigned off) {
  return __builtin_amdgcn_ds_read_tr16_b64_v4i16((lds_b4_ptr)(smem + off));
}
DI bf16x8 tr_read2(const char* smem, unsigned off0, unsigned off1) {
  bf16x4 lo = tr_read(smem, off0), hi = tr_read(smem, off1);
  return __builtin_shufflevector(lo, hi, 0, 1, 2, 3, 4, 5, 6, 7);
}
DI void glds16(const char* gbase, unsigned voff, unsigned ldst) {
  unsigned keep;
  asm volatile("s_mov_b32 %0, m0\n\ts_mov_b32 m0, %3\n\ts_nop 0\n\tglobal_load_lds_dwordx4 %1, %2\n\ts_mov_b32 m0, %0"
               : "=&s"(keep) : "v"(voff), "s"(gbase), "s"(ldst) : "memory");
}
DI bf16x8 lds128(const char* smem, unsigned off) { return *reinterpret_cast<const bf16x8*>(smem + off); }

DI int swzT(int row) { return ((row & 3) << 2) | ((((row >> 2) ^ (row >> 3)) & 1) << 1); }
DI int swap2(int p) { return ((p & 1) << 1) | (p >> 1); }

DI constexpr int slot_of(int c) {
  return c < 7 ? (c % 3) : (c == 7 ? 3 : (c == 8 ? 4 : ((c - 9) % 5 == 0 ? 1 : (c - 9) % 5 == 1 ? 2 : (c - 9) % 5 == 2 ? 0 : (c - 9) % 5 == 3 ? 3 : 4)));
}

__global__ __launch_bounds__(256, 1) void attn_kernel(
    const __hip_bfloat16* __restrict__ xA, const __hip_bfloat16* __restrict__ kA, const __hip_bfloat16* __restrict__ vA,
    const __hip_bfloat16* __restrict__ wqA, const __hip_bfloat16* __restrict__ xB, const __hip_bfloat16* __restrict__ kB,
    const __hip_bfloat16* __restrict__ vB, const __hip_bfloat16* __restrict__ wqB, __hip_bfloat16* __restrict__ Of) {
  extern __shared__ __attribute__((aligned(16))) char smem[];
  const int tid = threadIdx.x, lane = tid & 63;
  const int w = __builtin_amdgcn_readfirstlane(tid >> 6);
  const int bid = blockIdx.x, xcd = bid & 7, idx = bid >> 3;
  const int ph = xcd * 4 + (idx >> 3), p = ph >> 4, h = ph & 15, b = idx & 7;
  const char* xg = (const char*)(p ? xB : xA) + (size_t)b * SQ * DM * 2;
  const char* wg = (const char*)(p ? wqB : wqA) + (size_t)h * DH * 2;
  const char* kg = (const char*)(p ? kB : kA) + (size_t)(b * NH + h) * SK * DH * 2;
  const char* vg = (const char*)(p ? vB : vA) + (size_t)(b * NH + h) * SK * DH * 2;

  int r32 = lane & 31, hi = lane >> 5, i16 = lane & 15, g = lane >> 4;
  int blk = (lane >> 4) & 1, q4 = (lane & 15) >> 2, pp = lane & 3;
#define RELANE() do { int l_ = lane; asm volatile("" : "+v"(l_)); r32 = l_ & 31; hi = l_ >> 5; i16 = l_ & 15; g = l_ >> 4; \
    blk = (l_ >> 4) & 1; q4 = (l_ & 15) >> 2; pp = l_ & 3; } while (0)

  bf16x8 xb[28], x9[14];
  {
    const char* xr = xg + (size_t)(32 * w + r32) * (DM * 2) + hi * 16;
#pragma unroll
    for (int s = 0; s < 28; ++s) xb[s] = *reinterpret_cast<const bf16x8*>(xr + s * 32);
    const char* x9r = xg + (size_t)(128 + i16) * (DM * 2) + g * 16;
#pragma unroll
    for (int s = 0; s < 14; ++s) x9[s] = *reinterpret_cast<const bf16x8*>(x9r + s * 64);
  }

  const unsigned lds_base = (unsigned)(size_t)(lds_b4_ptr)smem;
  const unsigned voffK = (unsigned)((16 * w + hi) * 512 + ((r32 ^ hi) << 4));
  const unsigned voffV = (unsigned)((16 * w + hi) * 512 + ((r32 ^ (hi << 2)) << 4));
  const unsigned voffW = (unsigned)((16 * w + hi) * 8192 + ((r32 ^ (hi << 2)) << 4));
  auto issue = [&](int c) {
    const char* gb;
    if (c < 7) gb = wg + (size_t)c * 64 * (NH * DH * 2);
    else if (c < 15) gb = kg + (size_t)(c - 7) * SLOT;
    else gb = vg + (size_t)(c - 15) * SLOT;
    const bool isK = (c >= 7 && c < 15);
    const unsigned lb = lds_base + slot_of(c) * SLOT + w * 8192;
#pragma unroll
    for (int n = 0; n < 8; ++n) {
      const unsigned x = isK ? (unsigned)(2 * n) : (unsigned)((((2 * n) & 3) << 2) | ((((n >> 1) ^ (n >> 2)) & 1) << 1));
      const unsigned vo = (c < 7 ? voffW : isK ? voffK : voffV) ^ (x << 4);
      const char* gbn = gb + (size_t)n * (c < 7 ? 16384 : 1024);
      glds16(gbn, vo, lb + n * 1024);
    }
  };
  issue(0); issue(1); issue(2); issue(7); issue(8);

  f32x16 qacc[8];
  f32x4 q9acc[4];
#pragma unroll
  for (int d = 0; d < 8; ++d) qacc[d] = f32x16{};
#pragma unroll
  for (int d = 0; d < 4; ++d) q9acc[d] = f32x4{};

#pragma unroll
  for (int c = 0; c < 7; ++c) {
    if (c == 0) WAITV(32); else if (c < 3) WAITV(24); else WAITV(8);
    BARRIER();
    if (c >= 1 && c <= 4) issue(c + 2); else if (c >= 5) issue(c + 4);
    unsigned sb = slot_of(c) * SLOT; asm volatile("" : "+s"(sb));
#pragma unroll
    for (int ks = 0; ks < 4; ++ks) {
#pragma unroll
      for (int d = 0; d < 8; ++d) {
        const int row0 = 16 * ks + 8 * hi + q4, row1 = row0 + 4;
        const int sp = swap2(pp);
        const int ch = 4 * d + 2 * blk + (sp >> 1);
        bf16x8 a = tr_read2(smem, sb + row0 * 512 + ((ch ^ swzT(row0)) << 4) + 8 * (sp & 1),
                            sb + row1 * 512 + ((ch ^ swzT(row1)) << 4) + 8 * (sp & 1));
        qacc[d] = mfma32(a, xb[4 * c + ks], qacc[d]);
      }
    }
#pragma unroll
    for (int ks = 0; ks < 2; ++ks) {
#pragma unroll
      for (int d = 0; d < 4; ++d) {
        const int row0 = 32 * ks + 8 * g + q4, row1 = row0 + 4;
        const int ch = 8 * w + 2 * d + (pp >> 1);
        bf16x8 a = tr_read2(smem, sb + row0 * 512 + ((ch ^ swzT(row0)) << 4) + 8 * (pp & 1),
                            sb + row1 * 512 + ((ch ^ swzT(row1)) << 4) + 8 * (pp & 1));
        q9acc[d] = mfma16x32(a, x9[2 * c + ks], q9acc[d]);
      }
    }
  }
  bf16x8 Qf[16];
#pragma unroll
  for (int s = 0; s < 16; ++s) {
    const int d = s >> 1, o = 8 * (s & 1);
    Qf[s] = pack8(qacc[d][o], qacc[d][o + 1], qacc[d][o + 2], qacc[d][o + 3], qacc[d][o + 4], qacc[d][o + 5], qacc[d][o + 6], qacc[d][o + 7]);
  }
  BARRIER();
#pragma unroll
  for (int d = 0; d < 4; ++d) {
    u32x2 v = {pack2(q9acc[d][0], q9acc[d][1]), pack2(q9acc[d][2], q9acc[d][3])};
    const int dh = 64 * w + 16 * d + 4 * g;
    *reinterpret_cast<u32x2*>(smem + i16 * 512 + (((dh >> 3) ^ i16) << 4) + ((dh & 4) << 1)) = v;
  }

  RELANE();
  unsigned sp[8][2][8];
  unsigned s9p[8][2];
  float mrun = -3.0e38f, m9run = -3.0e38f;
  bf16x8 Q9f[8];
#pragma unroll
  for (int t = 0; t < 8; ++t) {
    const int c = 7 + t;
    if (t == 0) WAITV(48); else if (t == 1) WAITV(48); else WAITV(24);
    BARRIER();
    if (t == 0) {
#pragma unroll
      for (int s = 0; s < 8; ++s) Q9f[s] = lds128(smem, i16 * 512 + (((4 * s + g) ^ i16) << 4));
    } else if (t == 1) { issue(11); issue(12); }
    else issue(c + 4);
    unsigned sb = slot_of(c) * SLOT; asm volatile("" : "+s"(sb));
#pragma unroll
    for (int u = 0; u < 2; ++u) {
      f32x16 acc = f32x16{};
#pragma unroll
      for (int s = 0; s < 16; ++s) {
        bf16x8 a = lds128(smem, sb + (32 * u + r32) * 512 + (((2 * s + hi) ^ (r32 & 15)) << 4));
        acc = mfma32(a, Qf[s], acc);
      }
#pragma unroll
      for (int r = 0; r < 16; r += 2) {
        mrun = fmaxf(mrun, fmaxf(acc[r], acc[r + 1]));
        sp[t][u][r >> 1] = pack2(acc[r], acc[r + 1]);
      }
    }
    {
      f32x4 acc = f32x4{};
#pragma unroll
      for (int s = 0; s < 8; ++s) {
        bf16x8 a = lds128(smem, sb + (16 * w + i16) * 512 + (((4 * s + g) ^ i16) << 4));
        acc = mfma16x32(a, Q9f[s], acc);
      }
      m9run = fmaxf(m9run, fmaxf(fmaxf(acc[0], acc[1]), fmaxf(acc[2], acc[3])));
      s9p[t][0] = pack2(acc[0], acc[1]);
      s9p[t][1] = pack2(acc[2], acc[3]);
    }
  }

  constexpr float CS = 0.0625f * 1.4426950408889634f;
  bf16x8 pf[8][4];
  float inv_l;
  {
    float m = fmaxf(mrun, __shfl_xor(mrun, 32));
    m = __uint_as_float(pack2(m, m) & 0xffff0000u);
    const float mc = m * CS;
    float l = 0.f;
#pragma unroll
    for (int t = 0; t < 8; ++t)
#pragma unroll
      for (int u = 0; u < 2; ++u) {
        unsigned pk[8];
#pragma unroll
        for (int r = 0; r < 8; ++r) {
          const float e0 = __builtin_amdgcn_exp2f(fmaf(__uint_as_float(sp[t][u][r] << 16), CS, -mc));
          const float e1 = __builtin_amdgcn_exp2f(fmaf(__uint_as_float(sp[t][u][r] & 0xffff0000u), CS, -mc));
          l += e0 + e1;
          pk[r] = pack2(e0, e1);
        }
        u32x4 w0 = {pk[0], pk[1], pk[2], pk[3]}, w1 = {pk[4], pk[5], pk[6], pk[7]};
        pf[t][2 * u] = __builtin_bit_cast(bf16x8, w0);
        pf[t][2 * u + 1] = __builtin_bit_cast(bf16x8, w1);
      }
    l += __shfl_xor(l, 32);
    inv_l = 1.0f / l;
  }
  bf16x4 p9f[8];
  float m9, l9;
  {
    float m = fmaxf(m9run, __shfl_xor(m9run, 16));
    m = fmaxf(m, __shfl_xor(m, 32));
    m = __uint_as_float(pack2(m, m) & 0xffff0000u);
    const float mc = m * CS;
    float l = 0.f;
#pragma unroll
    for (int t = 0; t < 8; ++t) {
      unsigned pk[2];
#pragma unroll
      for (int r = 0; r < 2; ++r) {
        const float e0 = __builtin_amdgcn_exp2f(fmaf(__uint_as_float(s9p[t][r] << 16), CS, -mc));
        const float e1 = __builtin_amdgcn_exp2f(fmaf(__uint_as_float(s9p[t][r] & 0xffff0000u), CS, -mc));
        l += e0 + e1;
        pk[r] = pack2(e0, e1);
      }
      u32x2 v = {pk[0], pk[1]};
      p9f[t] = __builtin_bit_cast(bf16x4, v);
    }
    l += __shfl_xor(l, 16);
    l += __shfl_xor(l, 32);
    m9 = mc; l9 = l;
  }

  RELANE();
  f32x16 oacc[8];
  f32x4 o9acc[16];
#pragma unroll
  for (int d = 0; d < 8; ++d) oacc[d] = f32x16{};
#pragma unroll
  for (int d = 0; d < 16; ++d) o9acc[d] = f32x4{};
#pragma unroll
  for (int t = 0; t < 8; ++t) {
    const int c = 15 + t;
    if (t <= 4) WAITV(24); else if (t == 5) WAITV(16); else if (t == 6) WAITV(8); else WAITV(0);
    BARRIER();
    if (c + 4 <= 22) issue(c + 4);
    unsigned sb = slot_of(c) * SLOT; asm volatile("" : "+s"(sb));
#pragma unroll
    for (int kp = 0; kp < 4; ++kp) {
#pragma unroll
      for (int d = 0; d < 8; ++d) {
        const int row0 = 16 * kp + 4 * hi + q4, row1 = row0 + 8;
        const int sp = swap2(pp);
        const int ch = 4 * d + 2 * blk + (sp >> 1);
        bf16x8 a = tr_read2(smem, sb + row0 * 512 + ((ch ^ swzT(row0)) << 4) + 8 * (sp & 1),
                            sb + row1 * 512 + ((ch ^ swzT(row1)) << 4) + 8 * (sp & 1));
        oacc[d] = mfma32(a, pf[t][kp], oacc[d]);
      }
    }
#pragma unroll
    for (int d = 0; d < 16; ++d) {
      const int row = 16 * w + 4 * g + q4;
      const int ch = 2 * d + (pp >> 1);
      bf16x4 a = tr_read(smem, sb + row * 512 + ((ch ^ swzT(row)) << 4) + 8 * (pp & 1));
      o9acc[d] = mfma16x16(a, p9f[t], o9acc[d]);
    }
  }

  char* ofp = (char*)Of + (size_t)p * OF_PER_P * 2;
  {
    char* dst = ofp + ((size_t)(4 * b + w) * 256 + h * 16) * 1024 + lane * 16;
#pragma unroll
    for (int s = 0; s < 16; ++s) {
      const int d = s >> 1, o = 8 * (s & 1);
      bf16x8 v = pack8(oacc[d][o] * inv_l, oacc[d][o + 1] * inv_l, oacc[d][o + 2] * inv_l, oacc[d][o + 3] * inv_l,
                       oacc[d][o + 4] * inv_l, oacc[d][o + 5] * inv_l, oacc[d][o + 6] * inv_l, oacc[d][o + 7] * inv_l);
      *reinterpret_cast<bf16x8*>(dst + s * 1024) = v;
    }
  }
  RELANE();
  BARRIER();
  {
    float* st = reinterpret_cast<float*>(smem + 4 * 16 * 1040);
#pragma unroll
    for (int d = 0; d < 16; ++d)
      *reinterpret_cast<f32x4*>(smem + (w * 16 + i16) * 1040 + (16 * d + 4 * g) * 4) = o9acc[d];
    if (g == 0) { st[(w * 16 + i16) * 2] = m9; st[(w * 16 + i16) * 2 + 1] = l9; }
    BARRIER();
#pragma unroll
    for (int it = 0; it < 2; ++it) {
      const int item = tid + 256 * it;
      const int q = item >> 5, ck = item & 31;
      float mw[4], lw[4];
#pragma unroll
      for (int ww = 0; ww < 4; ++ww) { mw[ww] = st[(ww * 16 + q) * 2]; lw[ww] = st[(ww * 16 + q) * 2 + 1]; }
      const float M = fmaxf(fmaxf(mw[0], mw[1]), fmaxf(mw[2], mw[3]));
      float f[4], L = 0.f;
#pragma unroll
      for (int ww = 0; ww < 4; ++ww) { f[ww] = __builtin_amdgcn_exp2f(mw[ww] - M); L += f[ww] * lw[ww]; }
      const float il = 1.0f / L;
      float o[8];
#pragma unroll
      for (int j = 0; j < 8; ++j) o[j] = 0.f;
#pragma unroll
      for (int ww = 0; ww < 4; ++ww) {
        const f32x4 a = *reinterpret_cast<const f32x4*>(smem + (ww * 16 + q) * 1040 + ck * 32);
        const f32x4 c2 = *reinterpret_cast<const f32x4*>(smem + (ww * 16 + q) * 1040 + ck * 32 + 16);
#pragma unroll
        for (int j = 0; j < 4; ++j) { o[j] += f[ww] * a[j]; o[4 + j] += f[ww] * c2[j]; }
      }
      bf16x8 v = pack8(o[0] * il, o[1] * il, o[2] * il, o[3] * il, o[4] * il, o[5] * il, o[6] * il, o[7] * il);
      char* dst = ofp + ((size_t)(32 + (b >> 1)) * 256 + h * 16 + (ck >> 1)) * 1024 + (16 * (b & 1) + q + 32 * (ck & 1)) * 16;
      *reinterpret_cast<bf16x8*>(dst) = v;
    }
  }
}

constexpr int K2_WAVE_LDS = 16384;
__global__ __launch_bounds__(512, 2) void oproj_kernel(const __hip_bfloat16* __restrict__ Of, const __hip_bfloat16* __restrict__ woA,
                                                       const __hip_bfloat16* __restrict__ woB, __hip_bfloat16* __restrict__ out) {
  extern __shared__ __attribute__((aligned(16))) char smem[];
  const int tid = threadIdx.x, lane = tid & 63;
  const int w = __builtin_amdgcn_readfirstlane(tid >> 6);
  const int bid = blockIdx.x;
  const int p = bid / 126, rem = bid % 126, mg = rem / 7, ng = rem % 7;
  const char* of = (const char*)Of + (size_t)p * OF_PER_P * 2 + ((size_t)(2 * mg) * 256 + w * 32) * 1024 + lane * 16;
  const char* wo = (const char*)(p ? woB : woA) + (size_t)(w * 512) * (DM * 2) + ng * 128;
  const int hi = lane >> 5, blk = (lane >> 4) & 1, q4 = (lane & 15) >> 2, pp = lane & 3;
  char* lw = smem + w * K2_WAVE_LDS;

  auto issue = [&](int ck) {
#pragma unroll
    for (int n = 0; n < 8; ++n) {
      const int row = 8 * n + (lane >> 3);
      const int ch = (lane & 7) ^ (((row >> 1) & 1) << 2);
      __builtin_amdgcn_global_load_lds((const unsigned*)(wo + (size_t)(64 * ck + row) * (DM * 2) + ch * 16),
                                       (unsigned*)(lw + (ck & 1) * 8192 + n * 1024), 16, 0, 0);
    }
  };
  f32x16 acc[2][2];
#pragma unroll
  for (int a = 0; a < 2; ++a)
#pragma unroll
    for (int c = 0; c < 2; ++c) acc[a][c] = f32x16{};
  bf16x8 ob[2][2][4];
  auto loadO = [&](int ck, int buf) {
#pragma unroll
    for (int mt = 0; mt < 2; ++mt)
#pragma unroll
      for (int ks = 0; ks < 4; ++ks)
        ob[buf][mt][ks] = *reinterpret_cast<const bf16x8*>(of + ((size_t)mt * 256 + 4 * ck + ks) * 1024);
  };
  loadO(0, 0); issue(0);
#pragma unroll
  for (int ck = 0; ck < 8; ++ck) {
    if (ck + 1 < 8) { loadO(ck + 1, (ck + 1) & 1); issue(ck + 1); WAITV(16); } else WAITV(0);
    const unsigned sb = w * K2_WAVE_LDS + (ck & 1) * 8192;
#pragma unroll
    for (int ks = 0; ks < 4; ++ks) {
#pragma unroll
      for (int nt = 0; nt < 2; ++nt) {
        const int row0 = 16 * ks + 8 * hi + q4, row1 = row0 + 4;
        const int ch = 4 * nt + 2 * blk + (pp >> 1);
        bf16x8 a = tr_read2(smem, sb + row0 * 128 + ((ch ^ (((row0 >> 1) & 1) << 2)) << 4) + 8 * (pp & 1),
                            sb + row1 * 128 + ((ch ^ (((row1 >> 1) & 1) << 2)) << 4) + 8 * (pp & 1));
#pragma unroll
        for (int mt = 0; mt < 2; ++mt) acc[nt][mt] = mfma32(a, ob[ck & 1][mt][ks], acc[nt][mt]);
      }
    }
    asm volatile("s_waitcnt lgkmcnt(0)" ::: "memory");
  }
  __syncthreads();
  const int r32 = lane & 31;
#pragma unroll
  for (int nt = 0; nt < 2; ++nt)
#pragma unroll
    for (int mt = 0; mt < 2; ++mt)
#pragma unroll
      for (int qd = 0; qd < 4; ++qd) {
        f32x4 v = {acc[nt][mt][4 * qd], acc[nt][mt][4 * qd + 1], acc[nt][mt][4 * qd + 2], acc[nt][mt][4 * qd + 3]};
        *reinterpret_cast<f32x4*>(smem + ((size_t)w * 64 + 32 * mt + r32) * 272 + (32 * nt + 8 * qd + 4 * hi) * 4) = v;
      }
  __syncthreads();
  {
    const int m = tid >> 3, nc = tid & 7;
    float o[8];
#pragma unroll
    for (int j = 0; j < 8; ++j) o[j] = 0.f;
#pragma unroll
    for (int ww = 0; ww < 8; ++ww) {
      const f32x4 a = *reinterpret_cast<const f32x4*>(smem + ((size_t)ww * 64 + m) * 272 + nc * 32);
      const f32x4 c = *reinterpret_cast<const f32x4*>(smem + ((size_t)ww * 64 + m) * 272 + nc * 32 + 16);
#pragma unroll
      for (int j = 0; j < 4; ++j) { o[j] += a[j]; o[4 + j] += c[j]; }
    }
    const int mt = 2 * mg + (m >> 5), r = m & 31;
    int row;
    if (mt < 32) row = (mt >> 2) * SQ + (mt & 3) * 32 + r;
    else row = (2 * (mt - 32) + (r >> 4)) * SQ + 128 + (r & 15);
    bf16x8 v = pack8(o[0], o[1], o[2], o[3], o[4], o[5], o[6], o[7]);
    *reinterpret_cast<bf16x8*>((char*)out + ((size_t)row * (2 * DM) + p * DM + ng * 64 + nc * 8) * 2) = v;
  }
}

extern "C" void kernel_launch(void* const* d_in, const int* in_sizes, int n_in, void* d_out, int out_size, void* d_ws,
                              size_t ws_size, hipStream_t stream) {
  const __hip_bfloat16* xA = (const __hip_bfloat16*)d_in[0];
  const __hip_bfloat16* kA = (const __hip_bfloat16*)d_in[1];
  const __hip_bfloat16* vA = (const __hip_bfloat16*)d_in[2];
  const __hip_bfloat16* wqA = (const __hip_bfloat16*)d_in[3];
  const __hip_bfloat16* woA = (const __hip_bfloat16*)d_in[4];
  const __hip_bfloat16* xB = (const __hip_bfloat16*)d_in[5];
  const __hip_bfloat16* kB = (const __hip_bfloat16*)d_in[6];
  const __hip_bfloat16* vB = (const __hip_bfloat16*)d_in[7];
  const __hip_bfloat16* wqB = (const __hip_bfloat16*)d_in[8];
  const __hip_bfloat16* woB = (const __hip_bfloat16*)d_in[9];
  __hip_bfloat16* Of = (__hip_bfloat16*)d_ws;
  hipFuncSetAttribute((const void*)attn_kernel, hipFuncAttributeMaxDynamicSharedMemorySize, LDS_BYTES);
  hipFuncSetAttribute((const void*)oproj_kernel, hipFuncAttributeMaxDynamicSharedMemorySize, 8 * 64 * 272);
  attn_kernel<<<256, 256, LDS_BYTES, stream>>>(xA, kA, vA, wqA, xB, kB, vB, wqB, Of);
  oproj_kernel<<<252, 512, 8 * 64 * 272, stream>>>(Of, woA, woB, (__hip_bfloat16*)d_out);
}
```

```cpp
#include <hip/hip_runtime.h>
#include <hip/hip_bf16.h>
#include <stdint.h>

typedef short bf16x8 __attribute__((ext_vector_type(8)));
typedef short bf16x4 __attribute__((ext_vector_type(4)));
typedef float f32x16 __attribute__((ext_vector_type(16)));
typedef float f32x4 __attribute__((ext_vector_type(4)));
typedef unsigned u32x4 __attribute__((ext_vector_type(4)));
typedef unsigned u32x2 __attribute__((ext_vector_type(2)));
typedef __bf16 bf2_t __attribute__((ext_vector_type(2)));
typedef __attribute__((address_space(3))) bf16x4* lds_b4_ptr;

#define DI __device__ __forceinline__

constexpr int NB = 8, SQ = 144, DM = 448, NH = 16, SK = 512, DH = 256;
constexpr int SLOT = 32768;
constexpr int LDS_BYTES = 5 * SLOT;
constexpr size_t OF_PER_P = (size_t)36 * 256 * 512;

DI unsigned pack2(float a, float b) {
  bf2_t v = {(__bf16)a, (__bf16)b};
  return __builtin_bit_cast(unsigned, v);
}
DI bf16x8 pack8(float a0, float a1, float a2, float a3, float a4, float a5, float a6, float a7) {
  u32x4 w = {pack2(a0, a1), pack2(a2, a3), pack2(a4, a5), pack2(a6, a7)};
  return __builtin_bit_cast(bf16x8, w);
}
DI f32x16 mfma32(bf16x8 a, bf16x8 b, f32x16 c) { return __builtin_amdgcn_mfma_f32_32x32x16_bf16(a, b, c, 0, 0, 0); }
DI f32x4 mfma16x32(bf16x8 a, bf16x8 b, f32x4 c) { return __builtin_amdgcn_mfma_f32_16x16x32_bf16(a, b, c, 0, 0, 0); }
DI f32x4 mfma16x16(bf16x4 a, bf16x4 b, f32x4 c) { return __builtin_amdgcn_mfma_f32_16x16x16bf16_1k(a, b, c, 0, 0, 0); }

#define WAITV(n) asm volatile("s_waitcnt vmcnt(%0)" ::"n"(n) : "memory")
#define SB() __builtin_amdgcn_sched_barrier(0)
#define BARRIER() do { asm volatile("s_waitcnt lgkmcnt(0)" ::: "memory"); __builtin_amdgcn_s_barrier(); asm volatile("" ::: "memory"); } while (0)

DI bf16x4 tr_read(const char* smem, unsigned off) {
  return __builtin_amdgcn_ds_read_tr16_b64_v4i16((lds_b4_ptr)(smem + off));
}
DI bf16x8 tr_read2(const char* smem, unsigned off0, unsigned off1) {
  bf16x4 lo = tr_read(smem, off0), hi = tr_read(smem, off1);
  return __builtin_shufflevector(lo, hi, 0, 1, 2, 3, 4, 5, 6, 7);
}
DI void glds16(const char* gbase, unsigned voff, unsigned ldst) {
  unsigned keep;
  asm volatile("s_mov_b32 %0, m0\n\ts_mov_b32 m0, %3\n\ts_nop 0\n\tglobal_load_lds_dwordx4 %1, %2\n\ts_mov_b32 m0, %0"
               : "=&s"(keep) : "v"(voff), "s"(gbase), "s"(ldst) : "memory");
}
DI bf16x8 lds128(const char* smem, unsigned off) { return *reinterpret_cast<const bf16x8*>(smem + off); }

DI int swzT(int row) { return ((row & 3) << 2) | ((((row >> 2) ^ (row >> 3)) & 1) << 1); }
DI int swap2(int p) { return ((p & 1) << 1) | (p >> 1); }

DI constexpr int slot_of(int c) {
  return c < 7 ? (c % 3) : (c == 7 ? 3 : (c == 8 ? 4 : ((c - 9) % 5 == 0 ? 1 : (c - 9) % 5 == 1 ? 2 : (c - 9) % 5 == 2 ? 0 : (c - 9) % 5 == 3 ? 3 : 4)));
}

__global__ __launch_bounds__(256, 1) void attn_kernel(
    const __hip_bfloat16* __restrict__ xA, const __hip_bfloat16* __restrict__ kA, const __hip_bfloat16* __restrict__ vA,
    const __hip_bfloat16* __restrict__ wqA, const __hip_bfloat16* __restrict__ xB, const __hip_bfloat16* __restrict__ kB,
    const __hip_bfloat16* __restrict__ vB, const __hip_bfloat16* __restrict__ wqB, __hip_bfloat16* __restrict__ Of) {
  extern __shared__ __attribute__((aligned(16))) char smem[];
  const int tid = threadIdx.x, lane = tid & 63;
  const int w = __builtin_amdgcn_readfirstlane(tid >> 6);
  const int bid = blockIdx.x, xcd = bid & 7, idx = bid >> 3;
  const int ph = xcd * 4 + (idx >> 3), p = ph >> 4, h = ph & 15, b = idx & 7;
  const char* xg = (const char*)(p ? xB : xA) + (size_t)b * SQ * DM * 2;
  const char* wg = (const char*)(p ? wqB : wqA) + (size_t)h * DH * 2;
  const char* kg = (const char*)(p ? kB : kA) + (size_t)(b * NH + h) * SK * DH * 2;
  const char* vg = (const char*)(p ? vB : vA) + (size_t)(b * NH + h) * SK * DH * 2;

  int r32 = lane & 31, hi = lane >> 5, i16 = lane & 15, g = lane >> 4;
  int blk = (lane >> 4) & 1, q4 = (lane & 15) >> 2, pp = lane & 3;
#define RELANE() do { int l_ = lane; asm volatile("" : "+v"(l_)); r32 = l_ & 31; hi = l_ >> 5; i16 = l_ & 15; g = l_ >> 4; \
    blk = (l_ >> 4) & 1; q4 = (l_ & 15) >> 2; pp = l_ & 3; } while (0)

  bf16x8 xb[28], x9[14];
  {
    const char* xr = xg + (size_t)(32 * w + r32) * (DM * 2) + hi * 16;
#pragma unroll
    for (int s = 0; s < 28; ++s) xb[s] = *reinterpret_cast<const bf16x8*>(xr + s * 32);
    const char* x9r = xg + (size_t)(128 + i16) * (DM * 2) + g * 16;
#pragma unroll
    for (int s = 0; s < 14; ++s) x9[s] = *reinterpret_cast<const bf16x8*>(x9r + s * 64);
  }

  const unsigned lds_base = (unsigned)(size_t)(lds_b4_ptr)smem;
  const unsigned voffK = (unsigned)((16 * w + hi) * 512 + ((r32 ^ hi) << 4));
  const unsigned voffV = (unsigned)((16 * w + hi) * 512 + ((r32 ^ (hi << 2)) << 4));
  const unsigned voffW = (unsigned)((16 * w + hi) * 8192 + ((r32 ^ (hi << 2)) << 4));
  auto issue = [&](int c) {
    const char* gb;
    if (c < 7) gb = wg + (size_t)c * 64 * (NH * DH * 2);
    else if (c < 15) gb = kg + (size_t)(c - 7) * SLOT;
    else gb = vg + (size_t)(c - 15) * SLOT;
    const bool isK = (c >= 7 && c < 15);
    const unsigned lb = lds_base + slot_of(c) * SLOT + w * 8192;
#pragma unroll
    for (int n = 0; n < 8; ++n) {
      const unsigned x = isK ? (unsigned)(2 * n) : (unsigned)((((2 * n) & 3) << 2) | ((((n >> 1) ^ (n >> 2)) & 1) << 1));
      const unsigned vo = (c < 7 ? voffW : isK ? voffK : voffV) ^ (x << 4);
      const char* gbn = gb + (size_t)n * (c < 7 ? 16384 : 1024);
      glds16(gbn, vo, lb + n * 1024);
    }
  };
  issue(0); issue(1); issue(2); issue(7); issue(8);

  f32x16 qacc[8];
  f32x4 q9acc[4];
#pragma unroll
  for (int d = 0; d < 8; ++d) qacc[d] = f32x16{};
#pragma unroll
  for (int d = 0; d < 4; ++d) q9acc[d] = f32x4{};

#pragma unroll
  for (int c = 0; c < 7; ++c) {
    if (c == 0) WAITV(32); else if (c < 3) WAITV(24); else WAITV(8);
    BARRIER();
    unsigned sb = slot_of(c) * SLOT; asm volatile("" : "+s"(sb));
    bf16x8 fr[2][8];
    auto ldm = [&](bf16x8 (&f)[8], int ks) {
#pragma unroll
      for (int d = 0; d < 8; ++d) {
        const int row0 = 16 * ks + 8 * hi + q4, row1 = row0 + 4;
        const int sp = swap2(pp);
        const int ch = 4 * d + 2 * blk + (sp >> 1);
        f[d] = tr_read2(smem, sb + row0 * 512 + ((ch ^ swzT(row0)) << 4) + 8 * (sp & 1),
                        sb + row1 * 512 + ((ch ^ swzT(row1)) << 4) + 8 * (sp & 1));
      }
    };
    auto ld9 = [&](bf16x8 (&f)[8]) {
#pragma unroll
      for (int ks = 0; ks < 2; ++ks)
#pragma unroll
        for (int d = 0; d < 4; ++d) {
          const int row0 = 32 * ks + 8 * g + q4, row1 = row0 + 4;
          const int ch = 8 * w + 2 * d + (pp >> 1);
          f[4 * ks + d] = tr_read2(smem, sb + row0 * 512 + ((ch ^ swzT(row0)) << 4) + 8 * (pp & 1),
                                   sb + row1 * 512 + ((ch ^ swzT(row1)) << 4) + 8 * (pp & 1));
        }
    };
    ldm(fr[0], 0);
    if (c >= 1 && c <= 4) issue(c + 2); else if (c >= 5) issue(c + 4);
#pragma unroll
    for (int ks = 0; ks < 4; ++ks) {
      if (ks < 3) ldm(fr[(ks + 1) & 1], ks + 1); else ld9(fr[0]);
      SB();
#pragma unroll
      for (int d = 0; d < 8; ++d) qacc[d] = mfma32(fr[ks & 1][d], xb[4 * c + ks], qacc[d]);
      SB();
    }
#pragma unroll
    for (int ks = 0; ks < 2; ++ks)
#pragma unroll
      for (int d = 0; d < 4; ++d) q9acc[d] = mfma16x32(fr[0][4 * ks + d], x9[2 * c + ks], q9acc[d]);
  }
  bf16x8 Qf[16];
#pragma unroll
  for (int s = 0; s < 16; ++s) {
    const int d = s >> 1, o = 8 * (s & 1);
    Qf[s] = pack8(qacc[d][o], qacc[d][o + 1], qacc[d][o + 2], qacc[d][o + 3], qacc[d][o + 4], qacc[d][o + 5], qacc[d][o + 6], qacc[d][o + 7]);
  }
  BARRIER();
#pragma unroll
  for (int d = 0; d < 4; ++d) {
    u32x2 v = {pack2(q9acc[d][0], q9acc[d][1]), pack2(q9acc[d][2], q9acc[d][3])};
    const int dh = 64 * w + 16 * d + 4 * g;
    *reinterpret_cast<u32x2*>(smem + i16 * 512 + (((dh >> 3) ^ i16) << 4) + ((dh & 4) << 1)) = v;
  }

  RELANE();
  unsigned sp[8][2][8];
  unsigned s9p[8][2];
  float mrun = -3.0e38f, m9run = -3.0e38f;
  bf16x8 Q9f[8];
#pragma unroll
  for (int t = 0; t < 8; ++t) {
    const int c = 7 + t;
    if (t == 0) WAITV(48); else if (t == 1) WAITV(48); else WAITV(24);
    BARRIER();
    unsigned sb = slot_of(c) * SLOT; asm volatile("" : "+s"(sb));
    bf16x8 fr[2][8];
    auto ldm = [&](bf16x8 (&f)[8], int gi) {
#pragma unroll
      for (int s = 0; s < 8; ++s)
        f[s] = lds128(smem, sb + (32 * (gi >> 1) + r32) * 512 + (((2 * (8 * (gi & 1) + s) + hi) ^ (r32 & 15)) << 4));
    };
    auto ld9 = [&](bf16x8 (&f)[8]) {
#pragma unroll
      for (int s = 0; s < 8; ++s) f[s] = lds128(smem, sb + (16 * w + i16) * 512 + (((4 * s + g) ^ i16) << 4));
    };
    ldm(fr[0], 0);
    if (t == 0) {
#pragma unroll
      for (int s = 0; s < 8; ++s) Q9f[s] = lds128(smem, i16 * 512 + (((4 * s + g) ^ i16) << 4));
    } else if (t == 1) { issue(11); issue(12); }
    else issue(c + 4);
    f32x16 acc[2];
    acc[0] = f32x16{}; acc[1] = f32x16{};
#pragma unroll
    for (int gi = 0; gi < 4; ++gi) {
      if (gi < 3) ldm(fr[(gi + 1) & 1], gi + 1); else ld9(fr[0]);
      SB();
#pragma unroll
      for (int s = 0; s < 8; ++s) acc[gi >> 1] = mfma32(fr[gi & 1][s], Qf[8 * (gi & 1) + s], acc[gi >> 1]);
      SB();
    }
    {
      f32x4 a9 = f32x4{};
#pragma unroll
      for (int s = 0; s < 8; ++s) a9 = mfma16x32(fr[0][s], Q9f[s], a9);
#pragma unroll
      for (int u = 0; u < 2; ++u)
#pragma unroll
        for (int r = 0; r < 16; r += 2) {
          mrun = fmaxf(mrun, fmaxf(acc[u][r], acc[u][r + 1]));
          sp[t][u][r >> 1] = pack2(acc[u][r], acc[u][r + 1]);
        }
      m9run = fmaxf(m9run, fmaxf(fmaxf(a9[0], a9[1]), fmaxf(a9[2], a9[3])));
      s9p[t][0] = pack2(a9[0], a9[1]);
      s9p[t][1] = pack2(a9[2], a9[3]);
    }
  }

  constexpr float CS = 0.0625f * 1.4426950408889634f;
  bf16x8 pf[8][4];
  float inv_l;
  {
    float m = fmaxf(mrun, __shfl_xor(mrun, 32));
    m = __uint_as_float(pack2(m, m) & 0xffff0000u);
    const float mc = m * CS;
    float l = 0.f;
#pragma unroll
    for (int t = 0; t < 8; ++t)
#pragma unroll
      for (int u = 0; u < 2; ++u) {
        unsigned pk[8];
#pragma unroll
        for (int r = 0; r < 8; ++r) {
          const float e0 = __builtin_amdgcn_exp2f(fmaf(__uint_as_float(sp[t][u][r] << 16), CS, -mc));
          const float e1 = __builtin_amdgcn_exp2f(fmaf(__uint_as_float(sp[t][u][r] & 0xffff0000u), CS, -mc));
          l += e0 + e1;
          pk[r] = pack2(e0, e1);
        }
        u32x4 w0 = {pk[0], pk[1], pk[2], pk[3]}, w1 = {pk[4], pk[5], pk[6], pk[7]};
        pf[t][2 * u] = __builtin_bit_cast(bf16x8, w0);
        pf[t][2 * u + 1] = __builtin_bit_cast(bf16x8, w1);
      }
    l += __shfl_xor(l, 32);
    inv_l = 1.0f / l;
  }
  bf16x4 p9f[8];
  float m9, l9;
  {
    float m = fmaxf(m9run, __shfl_xor(m9run, 16));
    m = fmaxf(m, __shfl_xor(m, 32));
    m = __uint_as_float(pack2(m, m) & 0xffff0000u);
    const float mc = m * CS;
    float l = 0.f;
#pragma unroll
    for (int t = 0; t < 8; ++t) {
      unsigned pk[2];
#pragma unroll
      for (int r = 0; r < 2; ++r) {
        const float e0 = __builtin_amdgcn_exp2f(fmaf(__uint_as_float(s9p[t][r] << 16), CS, -mc));
        const float e1 = __builtin_amdgcn_exp2f(fmaf(__uint_as_float(s9p[t][r] & 0xffff0000u), CS, -mc));
        l += e0 + e1;
        pk[r] = pack2(e0, e1);
      }
      u32x2 v = {pk[0], pk[1]};
      p9f[t] = __builtin_bit_cast(bf16x4, v);
    }
    l += __shfl_xor(l, 16);
    l += __shfl_xor(l, 32);
    m9 = mc; l9 = l;
  }

  RELANE();
  f32x16 oacc[8];
  f32x4 o9acc[16];
#pragma unroll
  for (int d = 0; d < 8; ++d) oacc[d] = f32x16{};
#pragma unroll
  for (int d = 0; d < 16; ++d) o9acc[d] = f32x4{};
#pragma unroll
  for (int t = 0; t < 8; ++t) {
    const int c = 15 + t;
    if (t <= 4) WAITV(24); else if (t == 5) WAITV(16); else if (t == 6) WAITV(8); else WAITV(0);
    BARRIER();
    unsigned sb = slot_of(c) * SLOT; asm volatile("" : "+s"(sb));
    bf16x8 fr[2][8];
    auto ldm = [&](bf16x8 (&f)[8], int kp) {
#pragma unroll
      for (int d = 0; d < 8; ++d) {
        const int row0 = 16 * kp + 4 * hi + q4, row1 = row0 + 8;
        const int sp = swap2(pp);
        const int ch = 4 * d + 2 * blk + (sp >> 1);
        f[d] = tr_read2(smem, sb + row0 * 512 + ((ch ^ swzT(row0)) << 4) + 8 * (sp & 1),
                        sb + row1 * 512 + ((ch ^ swzT(row1)) << 4) + 8 * (sp & 1));
      }
    };
    auto ld9 = [&](bf16x8 (&f)[8]) {
#pragma unroll
      for (int d = 0; d < 8; ++d) {
        const int row = 16 * w + 4 * g + q4;
        const int ch0 = 4 * d + (pp >> 1), ch1 = ch0 + 2;
        f[d] = tr_read2(smem, sb + row * 512 + ((ch0 ^ swzT(row)) << 4) + 8 * (pp & 1),
                        sb + row * 512 + ((ch1 ^ swzT(row)) << 4) + 8 * (pp & 1));
      }
    };
    ldm(fr[0], 0);
    if (c + 4 <= 22) issue(c + 4);
#pragma unroll
    for (int kp = 0; kp < 4; ++kp) {
      if (kp < 3) ldm(fr[(kp + 1) & 1], kp + 1); else ld9(fr[0]);
      SB();
#pragma unroll
      for (int d = 0; d < 8; ++d) oacc[d] = mfma32(fr[kp & 1][d], pf[t][kp], oacc[d]);
      SB();
    }
#pragma unroll
    for (int d = 0; d < 8; ++d) {
      bf16x4 lo = __builtin_shufflevector(fr[0][d], fr[0][d], 0, 1, 2, 3), hi4 = __builtin_shufflevector(fr[0][d], fr[0][d], 4, 5, 6, 7);
      o9acc[2 * d] = mfma16x16(lo, p9f[t], o9acc[2 * d]);
      o9acc[2 * d + 1] = mfma16x16(hi4, p9f[t], o9acc[2 * d + 1]);
    }
  }

  char* ofp = (char*)Of + (size_t)p * OF_PER_P * 2;
  {
    char* dst = ofp + ((size_t)(4 * b + w) * 256 + h * 16) * 1024 + lane * 16;
#pragma unroll
    for (int s = 0; s < 16; ++s) {
      const int d = s >> 1, o = 8 * (s & 1);
      bf16x8 v = pack8(oacc[d][o] * inv_l, oacc[d][o + 1] * inv_l, oacc[d][o + 2] * inv_l, oacc[d][o + 3] * inv_l,
                       oacc[d][o + 4] * inv_l, oacc[d][o + 5] * inv_l, oacc[d][o + 6] * inv_l, oacc[d][o + 7] * inv_l);
      *reinterpret_cast<bf16x8*>(dst + s * 1024) = v;
    }
  }
  RELANE();
  BARRIER();
  {
    float* st = reinterpret_cast<float*>(smem + 4 * 16 * 1040);
#pragma unroll
    for (int d = 0; d < 16; ++d)
      *reinterpret_cast<f32x4*>(smem + (w * 16 + i16) * 1040 + (16 * d + 4 * g) * 4) = o9acc[d];
    if (g == 0) { st[(w * 16 + i16) * 2] = m9; st[(w * 16 + i16) * 2 + 1] = l9; }
    BARRIER();
#pragma unroll
    for (int it = 0; it < 2; ++it) {
      const int item = tid + 256 * it;
      const int q = item >> 5, ck = item & 31;
      float mw[4], lw[4];
#pragma unroll
      for (int ww = 0; ww < 4; ++ww) { mw[ww] = st[(ww * 16 + q) * 2]; lw[ww] = st[(ww * 16 + q) * 2 + 1]; }
      const float M = fmaxf(fmaxf(mw[0], mw[1]), fmaxf(mw[2], mw[3]));
      float f[4], L = 0.f;
#pragma unroll
      for (int ww = 0; ww < 4; ++ww) { f[ww] = __builtin_amdgcn_exp2f(mw[ww] - M); L += f[ww] * lw[ww]; }
      const float il = 1.0f / L;
      float o[8];
#pragma unroll
      for (int j = 0; j < 8; ++j) o[j] = 0.f;
#pragma unroll
      for (int ww = 0; ww < 4; ++ww) {
        const f32x4 a = *reinterpret_cast<const f32x4*>(smem + (ww * 16 + q) * 1040 + ck * 32);
        const f32x4 c2 = *reinterpret_cast<const f32x4*>(smem + (ww * 16 + q) * 1040 + ck * 32 + 16);
#pragma unroll
        for (int j = 0; j < 4; ++j) { o[j] += f[ww] * a[j]; o[4 + j] += f[ww] * c2[j]; }
      }
      bf16x8 v = pack8(o[0] * il, o[1] * il, o[2] * il, o[3] * il, o[4] * il, o[5] * il, o[6] * il, o[7] * il);
      char* dst = ofp + ((size_t)(32 + (b >> 1)) * 256 + h * 16 + (ck >> 1)) * 1024 + (16 * (b & 1) + q + 32 * (ck & 1)) * 16;
      *reinterpret_cast<bf16x8*>(dst) = v;
    }
  }
}

constexpr int K2_NBUF = 5;
constexpr int K2_WAVE_LDS = K2_NBUF * 4096;
DI bf16x8 gload16_asm(const char* p) {
  bf16x8 r;
  asm volatile("global_load_dwordx4 %0, %1, off" : "=v"(r) : "v"(p) : "memory");
  return r;
}
__global__ __launch_bounds__(512, 2) void oproj_kernel(const __hip_bfloat16* __restrict__ Of, const __hip_bfloat16* __restrict__ woA,
                                                       const __hip_bfloat16* __restrict__ woB, __hip_bfloat16* __restrict__ out) {
  extern __shared__ __attribute__((aligned(16))) char smem[];
  const int tid = threadIdx.x, lane = tid & 63;
  const int w = __builtin_amdgcn_readfirstlane(tid >> 6);
  const int bid = blockIdx.x;
  const int p = bid / 126, rem = bid % 126, mg = rem / 7, ng = rem % 7;
  const char* of = (const char*)Of + (size_t)p * OF_PER_P * 2 + ((size_t)(2 * mg) * 256 + w * 32) * 1024 + lane * 16;
  const char* wo = (const char*)(p ? woB : woA) + (size_t)(w * 512) * (DM * 2) + ng * 128;
  const int hi = lane >> 5, blk = (lane >> 4) & 1, q4 = (lane & 15) >> 2, pp = lane & 3;
  const unsigned lds_base = (unsigned)(size_t)(lds_b4_ptr)smem + w * K2_WAVE_LDS;
  const unsigned voff = (unsigned)((lane >> 3) * (DM * 2) + (((lane & 7) ^ (((lane >> 4) & 1) << 2)) << 4));

  bf16x8 ob[K2_NBUF][2][2];
  auto issue = [&](int ck) {
#pragma unroll
    for (int mt = 0; mt < 2; ++mt)
#pragma unroll
      for (int ks = 0; ks < 2; ++ks) ob[ck % K2_NBUF][mt][ks] = gload16_asm(of + ((size_t)mt * 256 + 2 * ck + ks) * 1024);
#pragma unroll
    for (int n = 0; n < 4; ++n)
      glds16(wo + (size_t)(32 * ck + 8 * n) * (DM * 2), voff, lds_base + (ck % K2_NBUF) * 4096 + n * 1024);
  };
  f32x16 acc[2][2];
#pragma unroll
  for (int a = 0; a < 2; ++a)
#pragma unroll
    for (int c = 0; c < 2; ++c) acc[a][c] = f32x16{};
  issue(0); issue(1); issue(2); issue(3);
#pragma unroll
  for (int ck = 0; ck < 16; ++ck) {
    if (ck + 4 < 16) { issue(ck + 4); WAITV(32); }
    else if (ck == 12) WAITV(24); else if (ck == 13) WAITV(16); else if (ck == 14) WAITV(8); else WAITV(0);
    {
      bf16x8 (&o)[2][2] = ob[ck % K2_NBUF];
      asm volatile("" : "+v"(o[0][0]), "+v"(o[0][1]), "+v"(o[1][0]), "+v"(o[1][1]));
    }
    const unsigned sb = w * K2_WAVE_LDS + (ck % K2_NBUF) * 4096;
    bf16x8 fa[2][2];
#pragma unroll
    for (int ks = 0; ks < 2; ++ks)
#pragma unroll
      for (int nt = 0; nt < 2; ++nt) {
        const int row0 = 16 * ks + 8 * hi + q4, row1 = row0 + 4;
        const int ch = 4 * nt + 2 * blk + (pp >> 1);
        fa[ks][nt] = tr_read2(smem, sb + row0 * 128 + ((ch ^ (((row0 >> 1) & 1) << 2)) << 4) + 8 * (pp & 1),
                              sb + row1 * 128 + ((ch ^ (((row1 >> 1) & 1) << 2)) << 4) + 8 * (pp & 1));
      }
#pragma unroll
    for (int ks = 0; ks < 2; ++ks)
#pragma unroll
      for (int nt = 0; nt < 2; ++nt)
#pragma unroll
        for (int mt = 0; mt < 2; ++mt) acc[nt][mt] = mfma32(fa[ks][nt], ob[ck % K2_NBUF][mt][ks], acc[nt][mt]);
    asm volatile("s_waitcnt lgkmcnt(0)" ::: "memory");
  }
  __syncthreads();
  const int r32 = lane & 31;
#pragma unroll
  for (int nt = 0; nt < 2; ++nt)
#pragma unroll
    for (int mt = 0; mt < 2; ++mt)
#pragma unroll
      for (int qd = 0; qd < 4; ++qd) {
        f32x4 v = {acc[nt][mt][4 * qd], acc[nt][mt][4 * qd + 1], acc[nt][mt][4 * qd + 2], acc[nt][mt][4 * qd + 3]};
        *reinterpret_cast<f32x4*>(smem + ((size_t)w * 64 + 32 * mt + r32) * 272 + (32 * nt + 8 * qd + 4 * hi) * 4) = v;
      }
  __syncthreads();
  {
    const int m = tid >> 3, nc = tid & 7;
    float o[8];
#pragma unroll
    for (int j = 0; j < 8; ++j) o[j] = 0.f;
#pragma unroll
    for (int ww = 0; ww < 8; ++ww) {
      const f32x4 a = *reinterpret_cast<const f32x4*>(smem + ((size_t)ww * 64 + m) * 272 + nc * 32);
      const f32x4 c = *reinterpret_cast<const f32x4*>(smem + ((size_t)ww * 64 + m) * 272 + nc * 32 + 16);
#pragma unroll
      for (int j = 0; j < 4; ++j) { o[j] += a[j]; o[4 + j] += c[j]; }
    }
    const int mt = 2 * mg + (m >> 5), r = m & 31;
    int row;
    if (mt < 32) row = (mt >> 2) * SQ + (mt & 3) * 32 + r;
    else row = (2 * (mt - 32) + (r >> 4)) * SQ + 128 + (r & 15);
    bf16x8 v = pack8(o[0], o[1], o[2], o[3], o[4], o[5], o[6], o[7]);
    *reinterpret_cast<bf16x8*>((char*)out + ((size_t)row * (2 * DM) + p * DM + ng * 64 + nc * 8) * 2) = v;
  }
}

extern "C" void kernel_launch(void* const* d_in, const int* in_sizes, int n_in, void* d_out, int out_size, void* d_ws,
                              size_t ws_size, hipStream_t stream) {
  const __hip_bfloat16* xA = (const __hip_bfloat16*)d_in[0];
  const __hip_bfloat16* kA = (const __hip_bfloat16*)d_in[1];
  const __hip_bfloat16* vA = (const __hip_bfloat16*)d_in[2];
  const __hip_bfloat16* wqA = (const __hip_bfloat16*)d_in[3];
  const __hip_bfloat16* woA = (const __hip_bfloat16*)d_in[4];
  const __hip_bfloat16* xB = (const __hip_bfloat16*)d_in[5];
  const __hip_bfloat16* kB = (const __hip_bfloat16*)d_in[6];
  const __hip_bfloat16* vB = (const __hip_bfloat16*)d_in[7];
  const __hip_bfloat16* wqB = (const __hip_bfloat16*)d_in[8];
  const __hip_bfloat16* woB = (const __hip_bfloat16*)d_in[9];
  __hip_bfloat16* Of = (__hip_bfloat16*)d_ws;
  hipFuncSetAttribute((const void*)attn_kernel, hipFuncAttributeMaxDynamicSharedMemorySize, LDS_BYTES);
  hipFuncSetAttribute((const void*)oproj_kernel, hipFuncAttributeMaxDynamicSharedMemorySize, 8 * K2_WAVE_LDS);
  attn_kernel<<<256, 256, LDS_BYTES, stream>>>(xA, kA, vA, wqA, xB, kB, vB, wqB, Of);
  oproj_kernel<<<252, 512, 8 * K2_WAVE_LDS, stream>>>(Of, woA, woB, (__hip_bfloat16*)d_out);
}
```

```cpp
#include <hip/hip_runtime.h>
#include <hip/hip_bf16.h>
#include <stdint.h>

typedef short bf16x8 __attribute__((ext_vector_type(8)));
typedef short bf16x4 __attribute__((ext_vector_type(4)));
typedef float f32x16 __attribute__((ext_vector_type(16)));
typedef float f32x4 __attribute__((ext_vector_type(4)));
typedef unsigned u32x4 __attribute__((ext_vector_type(4)));
typedef unsigned u32x2 __attribute__((ext_vector_type(2)));
typedef __bf16 bf2_t __attribute__((ext_vector_type(2)));
typedef __attribute__((address_space(3))) bf16x4* lds_b4_ptr;

#define DI __device__ __forceinline__

constexpr int NB = 8, SQ = 144, DM = 448, NH = 16, SK = 512, DH = 256;
constexpr int SLOT = 32768;
constexpr int LDS_BYTES = 5 * SLOT;
constexpr size_t OF_PER_P = (size_t)36 * 256 * 512;

DI unsigned pack2(float a, float b) {
  bf2_t v = {(__bf16)a, (__bf16)b};
  return __builtin_bit_cast(unsigned, v);
}
DI bf16x8 pack8(float a0, float a1, float a2, float a3, float a4, float a5, float a6, float a7) {
  u32x4 w = {pack2(a0, a1), pack2(a2, a3), pack2(a4, a5), pack2(a6, a7)};
  return __builtin_bit_cast(bf16x8, w);
}
DI f32x16 mfma32(bf16x8 a, bf16x8 b, f32x16 c) { return __builtin_amdgcn_mfma_f32_32x32x16_bf16(a, b, c, 0, 0, 0); }
DI f32x4 mfma16x32(bf16x8 a, bf16x8 b, f32x4 c) { return __builtin_amdgcn_mfma_f32_16x16x32_bf16(a, b, c, 0, 0, 0); }
DI f32x4 mfma16x16(bf16x4 a, bf16x4 b, f32x4 c) { return __builtin_amdgcn_mfma_f32_16x16x16bf16_1k(a, b, c, 0, 0, 0); }

#define WAITV(n) asm volatile("s_waitcnt vmcnt(%0)" ::"n"(n) : "memory")
#define SB() __builtin_amdgcn_sched_barrier(0)
#define STAMP(i)
#define BARRIER() do { asm volatile("s_waitcnt lgkmcnt(0)" ::: "memory"); __builtin_amdgcn_s_barrier(); asm volatile("" ::: "memory"); } while (0)

DI bf16x4 tr_read(const char* smem, unsigned off) {
  return __builtin_amdgcn_ds_read_tr16_b64_v4i16((lds_b4_ptr)(smem + off));
}
DI bf16x8 tr_read2(const char* smem, unsigned off0, unsigned off1) {
  bf16x4 lo = tr_read(smem, off0), hi = tr_read(smem, off1);
  return __builtin_shufflevector(lo, hi, 0, 1, 2, 3, 4, 5, 6, 7);
}
DI void glds16(const char* gbase, unsigned voff, unsigned ldst) {
  unsigned keep;
  asm volatile("s_mov_b32 %0, m0\n\ts_mov_b32 m0, %3\n\ts_nop 0\n\tglobal_load_lds_dwordx4 %1, %2\n\ts_mov_b32 m0, %0"
               : "=&s"(keep) : "v"(voff), "s"(gbase), "s"(ldst) : "memory");
}
DI bf16x8 lds128(const char* smem, unsigned off) { return *reinterpret_cast<const bf16x8*>(smem + off); }

typedef __attribute__((address_space(3))) bf16x8* lds_b8_ptr;
DI bf16x4 trA(unsigned addr) { return __builtin_amdgcn_ds_read_tr16_b64_v4i16(reinterpret_cast<lds_b4_ptr>(addr)); }
DI bf16x8 trA2(unsigned a0, unsigned a1) {
  bf16x4 lo = trA(a0), hi = trA(a1);
  return __builtin_shufflevector(lo, hi, 0, 1, 2, 3, 4, 5, 6, 7);
}
DI bf16x8 ldA(unsigned addr) { return *reinterpret_cast<lds_b8_ptr>(addr); }
DI void ascale(float& a, float s) {
  float tmp;
  asm volatile("v_accvgpr_read_b32 %1, %0\n\tv_mul_f32 %1, %1, %2\n\tv_accvgpr_write_b32 %0, %1" : "+a"(a), "=&v"(tmp) : "v"(s));
}
#define OPAQUE(x) asm volatile("" : "+v"(x))

DI int swzT(int row) { return ((row & 3) << 2) | ((((row >> 2) ^ (row >> 3)) & 1) << 1); }
DI int swap2(int p) { return ((p & 1) << 1) | (p >> 1); }

constexpr int STG = 53248;

__global__ __launch_bounds__(256, 1) void attn_kernel(
    const __hip_bfloat16* __restrict__ xA, const __hip_bfloat16* __restrict__ kA, const __hip_bfloat16* __restrict__ vA,
    const __hip_bfloat16* __restrict__ wqA, const __hip_bfloat16* __restrict__ xB, const __hip_bfloat16* __restrict__ kB,
    const __hip_bfloat16* __restrict__ vB, const __hip_bfloat16* __restrict__ wqB, __hip_bfloat16* __restrict__ Of
    ) {
  extern __shared__ __attribute__((aligned(16))) char smem[];
  const int tid = threadIdx.x, lane = tid & 63;
  const int w = __builtin_amdgcn_readfirstlane(tid >> 6);
  const int bid = blockIdx.x, xcd = bid & 7, idx = bid >> 3;
  const int ph = xcd * 4 + (idx >> 3), p = ph >> 4, h = ph & 15, b = idx & 7;
  const char* xg = (const char*)(p ? xB : xA) + (size_t)b * SQ * DM * 2;
  const char* wg = (const char*)(p ? wqB : wqA) + (size_t)h * DH * 2;
  const char* kg = (const char*)(p ? kB : kA) + (size_t)(b * NH + h) * SK * DH * 2;
  const char* vg = (const char*)(p ? vB : vA) + (size_t)(b * NH + h) * SK * DH * 2;

  int r32 = lane & 31, hi = lane >> 5, i16 = lane & 15, g = lane >> 4;
  int blk = (lane >> 4) & 1, q4 = (lane & 15) >> 2, pp = lane & 3;
#define RELANE() do { int l_ = lane; asm volatile("" : "+v"(l_)); r32 = l_ & 31; hi = l_ >> 5; i16 = l_ & 15; g = l_ >> 4; \
    blk = (l_ >> 4) & 1; q4 = (l_ & 15) >> 2; pp = l_ & 3; } while (0)

  const unsigned lds_base = (unsigned)(size_t)(lds_b4_ptr)smem;
  const unsigned voffK = (unsigned)((16 * w + hi) * 512 + ((r32 ^ hi) << 4));
  const unsigned voffV = (unsigned)((16 * w + hi) * 512 + ((r32 ^ (hi << 2)) << 4));
  const unsigned voffW = (unsigned)((16 * w + hi) * 8192 + ((r32 ^ (hi << 2)) << 4));
  unsigned voffX[5];
#pragma unroll
  for (int n = 0; n < 5; ++n) {
    const int gi = 5 * w + n;
    int row = 8 * gi + (lane >> 3);
    const int ch = (lane & 7) ^ ((row >> 1) & 7);
    row = row < SQ ? row : SQ - 1;
    voffX[n] = (unsigned)(row * (DM * 2) + ch * 16);
  }
  auto issueKV = [&](const char* gb, bool isK, unsigned ldst) {
    unsigned vb = isK ? voffK : voffV; OPAQUE(vb);
#pragma unroll
    for (int n = 0; n < 8; ++n) {
      const unsigned x = isK ? (unsigned)(2 * n) : (unsigned)((((2 * n) & 3) << 2) | ((((n >> 1) ^ (n >> 2)) & 1) << 1));
      glds16(gb + n * 1024, vb ^ (x << 4), lds_base + ldst + w * 8192 + n * 1024);
    }
  };
  auto issueQ = [&](int c, unsigned ldst) {
    unsigned vb = voffW; OPAQUE(vb);
#pragma unroll
    for (int n = 0; n < 8; ++n) {
      const unsigned x = (unsigned)((((2 * n) & 3) << 2) | ((((n >> 1) ^ (n >> 2)) & 1) << 1));
      glds16(wg + (size_t)c * (64 * NH * DH * 2) + n * 16384, vb ^ (x << 4), lds_base + ldst + w * 8192 + n * 1024);
    }
#pragma unroll
    for (int n = 0; n < 5; ++n) glds16(xg + c * 128, voffX[n], lds_base + ldst + SLOT + (5 * w + n) * 1024);
  };
  STAMP(0);
  issueQ(0, 0); issueQ(1, STG);

  f32x16 qacc[8];
  f32x4 q9acc[4];
#pragma unroll
  for (int d = 0; d < 8; ++d) qacc[d] = f32x16{};
#pragma unroll
  for (int d = 0; d < 4; ++d) q9acc[d] = f32x4{};

  {
    const int spw = swap2(pp);
    const unsigned laneW = (unsigned)((8 * hi + q4) * 512 + ((((q4 << 2) | ((blk ^ hi) << 1)) ^ (spw >> 1)) << 4) + 8 * (spw & 1));
    const unsigned lane9 = (unsigned)((8 * g + q4) * 512 + (((8 * w + (pp >> 1)) ^ ((q4 << 2) | ((g & 1) << 1))) << 4) + 8 * (pp & 1));
    const unsigned laneX = (unsigned)(SLOT + (32 * w + r32) * 128 + ((hi ^ ((r32 >> 1) & 7)) << 4));
    const unsigned laneX9 = (unsigned)(SLOT + (128 + i16) * 128 + ((g ^ ((i16 >> 1) & 7)) << 4));
    unsigned stg = 0;
#pragma unroll 1
    for (int c = 0; c < 7; ++c) {
      STAMP(1 + 2 * c);
      if (c < 6) WAITV(13); else WAITV(8);
      BARRIER();
      STAMP(2 + 2 * c);
      const unsigned sb = stg;
      bf16x8 fr[2][8], xq[4], x9q[2];
      unsigned bW0 = lds_base + sb + laneW; OPAQUE(bW0);
      unsigned bX = lds_base + sb + laneX; OPAQUE(bX);
      unsigned bX9 = lds_base + sb + laneX9; OPAQUE(bX9);
      const unsigned bW1 = (bW0 ^ 32) + 2048;
      unsigned b9 = lds_base + sb + lane9; OPAQUE(b9);
      auto ldm = [&](bf16x8 (&f)[8], int ks) {
#pragma unroll
        for (int d = 0; d < 8; ++d)
          f[d] = trA2((bW0 ^ ((d & 3) << 6)) + (d >> 2) * 256 + ks * 8192, (bW1 ^ ((d & 3) << 6)) + (d >> 2) * 256 + ks * 8192);
      };
      auto ld9 = [&](bf16x8 (&f)[8]) {
#pragma unroll
        for (int ks = 0; ks < 2; ++ks)
#pragma unroll
          for (int d = 0; d < 4; ++d)
            f[4 * ks + d] = trA2((b9 ^ (d << 5)) + ks * 16384, ((b9 ^ 32) ^ (d << 5)) + 2048 + ks * 16384);
      };
#pragma unroll
      for (int ks = 0; ks < 4; ++ks) {
        xq[ks] = ldA(bX ^ (ks << 5));
      }
      ldm(fr[0], 0);
#pragma unroll
      for (int ks = 0; ks < 2; ++ks) {
        x9q[ks] = ldA(bX9 ^ (ks << 6));
      }
      {
        unsigned nx = stg + 2 * STG; nx = nx >= 3 * STG ? nx - 3 * STG : nx;
        if (c <= 4) issueQ(c + 2, nx);
        else if (c == 5) issueKV(kg, true, 2 * SLOT);
        else { issueKV(vg, false, 3 * SLOT); issueKV(kg + SLOT, true, 4 * SLOT); }
      }
#pragma unroll
      for (int ks = 0; ks < 4; ++ks) {
        if (ks < 3) ldm(fr[(ks + 1) & 1], ks + 1); else ld9(fr[0]);
        SB();
#pragma unroll
        for (int d = 0; d < 8; ++d) qacc[d] = mfma32(fr[ks & 1][d], xq[ks], qacc[d]);
        SB();
      }
#pragma unroll
      for (int ks = 0; ks < 2; ++ks)
#pragma unroll
        for (int d = 0; d < 4; ++d) q9acc[d] = mfma16x32(fr[0][4 * ks + d], x9q[ks], q9acc[d]);
      stg += STG; stg = stg >= 3 * STG ? 0 : stg;
    }
  }
  bf16x8 Qf[16];
#pragma unroll
  for (int s = 0; s < 16; ++s) {
    const int d = s >> 1, o = 8 * (s & 1);
    Qf[s] = pack8(qacc[d][o], qacc[d][o + 1], qacc[d][o + 2], qacc[d][o + 3], qacc[d][o + 4], qacc[d][o + 5], qacc[d][o + 6], qacc[d][o + 7]);
  }
  RELANE();
  BARRIER();
#pragma unroll
  for (int d = 0; d < 4; ++d) {
    u32x2 v = {pack2(q9acc[d][0], q9acc[d][1]), pack2(q9acc[d][2], q9acc[d][3])};
    const int dh = 64 * w + 16 * d + 4 * g;
    *reinterpret_cast<u32x2*>(smem + SLOT + i16 * 512 + (((dh >> 3) ^ i16) << 4) + ((dh & 4) << 1)) = v;
  }

  constexpr float CS = 0.0625f * 1.4426950408889634f;
  constexpr float THR = 8.0f / CS;
  f32x16 oacc[8];
  f32x4 o9acc[16];
#pragma unroll
  for (int d = 0; d < 8; ++d) oacc[d] = f32x16{};
#pragma unroll
  for (int d = 0; d < 16; ++d) o9acc[d] = f32x4{};
  float m = -3.0e38f, l = 0.f, m9 = -3.0e38f, l9 = 0.f;
  bf16x8 Q9f[8];
  BARRIER();
#pragma unroll
  for (int s = 0; s < 8; ++s) Q9f[s] = ldA(lds_base + SLOT + i16 * 512 + (((4 * s + g) ^ i16) << 4));
  const unsigned laneK = (unsigned)(r32 * 512 + ((hi ^ (r32 & 15)) << 4));
  const unsigned laneK9 = (unsigned)((16 * w + i16) * 512 + ((g ^ i16) << 4));
  const int spv = swap2(pp);
  const unsigned laneV = (unsigned)((4 * hi + q4) * 512 + ((((q4 << 2) | ((blk ^ hi) << 1)) ^ (spv >> 1)) << 4) + 8 * (spv & 1));
  const unsigned laneV9 = (unsigned)((16 * w + 4 * g + q4) * 512 + (((pp >> 1) ^ ((q4 << 2) | (((g ^ (g >> 1)) & 1) << 1))) << 4) + 8 * (pp & 1));
  int sl = 2;
#pragma unroll 1
  for (int t = 0; t < 8; ++t) {
    STAMP(15 + 2 * (t));
    if (t == 0) WAITV(16); else if (t < 7) WAITV(24); else WAITV(8 * 1);
    BARRIER();
    STAMP(16 + 2 * (t));
    f32x16 acc[2];
    f32x4 a9 = f32x4{};
    {
      const unsigned sb = sl * SLOT;
      bf16x8 fr[2][8];
      unsigned bK = lds_base + sb + laneK; OPAQUE(bK);
      unsigned bK9 = lds_base + sb + laneK9; OPAQUE(bK9);
      auto ldm = [&](bf16x8 (&f)[8], int gi) {
#pragma unroll
        for (int s = 0; s < 8; ++s) f[s] = ldA((bK ^ (s << 5)) + (gi & 1) * 256 + (gi >> 1) * 16384);
      };
      auto ld9 = [&](bf16x8 (&f)[8]) {
#pragma unroll
        for (int s = 0; s < 8; ++s) f[s] = ldA((bK9 ^ ((s & 3) << 6)) + (s >> 2) * 256);
      };
      ldm(fr[0], 0);
      {
        int ps = sl == 0 ? 4 : sl - 1;
        if (t == 0) issueKV(vg + SLOT, false, 0);
        else if (t < 6) issueKV(kg + (size_t)(t + 2) * SLOT, true, ps * SLOT);
      }
      acc[0] = f32x16{}; acc[1] = f32x16{};
#pragma unroll
      for (int gi = 0; gi < 4; ++gi) {
        if (gi < 3) ldm(fr[(gi + 1) & 1], gi + 1); else ld9(fr[0]);
        SB();
#pragma unroll
        for (int s = 0; s < 8; ++s) acc[gi >> 1] = mfma32(fr[gi & 1][s], Qf[8 * (gi & 1) + s], acc[gi >> 1]);
        SB();
      }
#pragma unroll
      for (int s = 0; s < 8; ++s) a9 = mfma16x32(fr[0][s], Q9f[s], a9);
      sl = sl == 4 ? 0 : sl + 1;
    }
    bf16x8 pf[4];
    bf16x4 p9f;
    {
      float mt = acc[0][0];
#pragma unroll
      for (int u = 0; u < 2; ++u)
#pragma unroll
        for (int r = 0; r < 16; ++r) mt = fmaxf(mt, acc[u][r]);
      mt = fmaxf(mt, __shfl_xor(mt, 32));
      float mt9 = fmaxf(fmaxf(a9[0], a9[1]), fmaxf(a9[2], a9[3]));
      mt9 = fmaxf(mt9, __shfl_xor(mt9, 16));
      mt9 = fmaxf(mt9, __shfl_xor(mt9, 32));
      if (__any((mt - m > THR) || (mt9 - m9 > THR))) {
        const float mn = fmaxf(m, mt), mn9 = fmaxf(m9, mt9);
        const float al = __builtin_amdgcn_exp2f((m - mn) * CS), al9 = __builtin_amdgcn_exp2f((m9 - mn9) * CS);
        m = mn; m9 = mn9; l *= al; l9 *= al9;
#pragma unroll
        for (int d = 0; d < 8; ++d)
#pragma unroll
          for (int r = 0; r < 16; ++r) { float t_ = oacc[d][r]; ascale(t_, al); oacc[d][r] = t_; }
#pragma unroll
        for (int d = 0; d < 16; ++d)
#pragma unroll
          for (int r = 0; r < 4; ++r) { float t_ = o9acc[d][r]; ascale(t_, al9); o9acc[d][r] = t_; }
      }
      const float mc = m * CS, mc9 = m9 * CS;
#pragma unroll
      for (int u = 0; u < 2; ++u) {
        unsigned pk[8];
#pragma unroll
        for (int r = 0; r < 8; ++r) {
          const float e0 = __builtin_amdgcn_exp2f(fmaf(acc[u][2 * r], CS, -mc));
          const float e1 = __builtin_amdgcn_exp2f(fmaf(acc[u][2 * r + 1], CS, -mc));
          l += e0 + e1;
          pk[r] = pack2(e0, e1);
        }
        u32x4 w0 = {pk[0], pk[1], pk[2], pk[3]}, w1 = {pk[4], pk[5], pk[6], pk[7]};
        pf[2 * u] = __builtin_bit_cast(bf16x8, w0);
        pf[2 * u + 1] = __builtin_bit_cast(bf16x8, w1);
      }
      const float e0 = __builtin_amdgcn_exp2f(fmaf(a9[0], CS, -mc9)), e1 = __builtin_amdgcn_exp2f(fmaf(a9[1], CS, -mc9));
      const float e2 = __builtin_amdgcn_exp2f(fmaf(a9[2], CS, -mc9)), e3 = __builtin_amdgcn_exp2f(fmaf(a9[3], CS, -mc9));
      l9 += (e0 + e1) + (e2 + e3);
      u32x2 v = {pack2(e0, e1), pack2(e2, e3)};
      p9f = __builtin_bit_cast(bf16x4, v);
    }
    STAMP(31 + 2 * (t));
    if (t == 0) WAITV(16); else if (t < 6) WAITV(24); else if (t == 6) WAITV(16); else WAITV(0);
    BARRIER();
    STAMP(32 + 2 * (t));
    {
      const unsigned sb = sl * SLOT;
      bf16x8 fr[2][8];
      unsigned bV0 = lds_base + sb + laneV; OPAQUE(bV0);
      const unsigned bV1 = (bV0 ^ 32) + 4096;
      unsigned bV9 = lds_base + sb + laneV9; OPAQUE(bV9);
      auto ldm = [&](bf16x8 (&f)[8], int kp) {
#pragma unroll
        for (int d = 0; d < 8; ++d)
          f[d] = trA2((bV0 ^ ((d & 3) << 6)) + (d >> 2) * 256 + kp * 8192, (bV1 ^ ((d & 3) << 6)) + (d >> 2) * 256 + kp * 8192);
      };
      auto ld9 = [&](bf16x8 (&f)[8]) {
#pragma unroll
        for (int d = 0; d < 8; ++d)
          f[d] = trA2((bV9 ^ ((d & 3) << 6)) + (d >> 2) * 256, ((bV9 ^ 32) ^ ((d & 3) << 6)) + (d >> 2) * 256);
      };
      ldm(fr[0], 0);
      {
        int ps = sl == 0 ? 4 : sl - 1;
        if (t == 0) { issueKV(kg + 2 * SLOT, true, 1 * SLOT); issueKV(vg + 2 * SLOT, false, ps * SLOT); }
        else if (t < 6) issueKV(vg + (size_t)(t + 2) * SLOT, false, ps * SLOT);
      }
#pragma unroll
      for (int kp = 0; kp < 4; ++kp) {
        if (kp < 3) ldm(fr[(kp + 1) & 1], kp + 1); else ld9(fr[0]);
        SB();
#pragma unroll
        for (int d = 0; d < 8; ++d) oacc[d] = mfma32(fr[kp & 1][d], pf[kp], oacc[d]);
        SB();
      }
#pragma unroll
      for (int d = 0; d < 8; ++d) {
        bf16x4 lo = __builtin_shufflevector(fr[0][d], fr[0][d], 0, 1, 2, 3), hi4 = __builtin_shufflevector(fr[0][d], fr[0][d], 4, 5, 6, 7);
        o9acc[2 * d] = mfma16x16(lo, p9f, o9acc[2 * d]);
        o9acc[2 * d + 1] = mfma16x16(hi4, p9f, o9acc[2 * d + 1]);
      }
      sl = sl == 4 ? 0 : sl + 1;
    }
  }
  l += __shfl_xor(l, 32);
  l9 += __shfl_xor(l9, 16);
  l9 += __shfl_xor(l9, 32);
  const float inv_l = 1.0f / l;

  STAMP(47);
  char* ofp = (char*)Of + (size_t)p * OF_PER_P * 2;
  {
    char* dst = ofp + ((size_t)(4 * b + w) * 256 + h * 16) * 1024 + lane * 16;
#pragma unroll
    for (int s = 0; s < 16; ++s) {
      const int d = s >> 1, o = 8 * (s & 1);
      bf16x8 v = pack8(oacc[d][o] * inv_l, oacc[d][o + 1] * inv_l, oacc[d][o + 2] * inv_l, oacc[d][o + 3] * inv_l,
                       oacc[d][o + 4] * inv_l, oacc[d][o + 5] * inv_l, oacc[d][o + 6] * inv_l, oacc[d][o + 7] * inv_l);
      *reinterpret_cast<bf16x8*>(dst + s * 1024) = v;
    }
  }
  RELANE();
  BARRIER();
  {
    float* st = reinterpret_cast<float*>(smem + 4 * 16 * 1040);
#pragma unroll
    for (int d = 0; d < 16; ++d)
      *reinterpret_cast<f32x4*>(smem + (w * 16 + i16) * 1040 + (16 * d + 4 * g) * 4) = o9acc[d];
    if (g == 0) { st[(w * 16 + i16) * 2] = m9 * CS; st[(w * 16 + i16) * 2 + 1] = l9; }
    BARRIER();
#pragma unroll
    for (int it = 0; it < 2; ++it) {
      const int item = tid + 256 * it;
      const int q = item >> 5, ck = item & 31;
      float mw[4], lw[4];
#pragma unroll
      for (int ww = 0; ww < 4; ++ww) { mw[ww] = st[(ww * 16 + q) * 2]; lw[ww] = st[(ww * 16 + q) * 2 + 1]; }
      const float M = fmaxf(fmaxf(mw[0], mw[1]), fmaxf(mw[2], mw[3]));
      float f[4], L = 0.f;
#pragma unroll
      for (int ww = 0; ww < 4; ++ww) { f[ww] = __builtin_amdgcn_exp2f(mw[ww] - M); L += f[ww] * lw[ww]; }
      const float il = 1.0f / L;
      float o[8];
#pragma unroll
      for (int j = 0; j < 8; ++j) o[j] = 0.f;
#pragma unroll
      for (int ww = 0; ww < 4; ++ww) {
        const f32x4 a = *reinterpret_cast<const f32x4*>(smem + (ww * 16 + q) * 1040 + ck * 32);
        const f32x4 c2 = *reinterpret_cast<const f32x4*>(smem + (ww * 16 + q) * 1040 + ck * 32 + 16);
#pragma unroll
        for (int j = 0; j < 4; ++j) { o[j] += f[ww] * a[j]; o[4 + j] += f[ww] * c2[j]; }
      }
      bf16x8 v = pack8(o[0] * il, o[1] * il, o[2] * il, o[3] * il, o[4] * il, o[5] * il, o[6] * il, o[7] * il);
      char* dst = ofp + ((size_t)(32 + (b >> 1)) * 256 + h * 16 + (ck >> 1)) * 1024 + (16 * (b & 1) + q + 32 * (ck & 1)) * 16;
      *reinterpret_cast<bf16x8*>(dst) = v;
    }
  }
  STAMP(48);
}

constexpr int K2_NBUF = 5;
constexpr int K2_WAVE_LDS = K2_NBUF * 4096;
DI bf16x8 gload16_asm(const char* p) {
  bf16x8 r;
  asm volatile("global_load_dwordx4 %0, %1, off" : "=v"(r) : "v"(p) : "memory");
  return r;
}
__global__ __launch_bounds__(512, 2) void oproj_kernel(const __hip_bfloat16* __restrict__ Of, const __hip_bfloat16* __restrict__ woA,
                                                       const __hip_bfloat16* __restrict__ woB, __hip_bfloat16* __restrict__ out) {
  extern __shared__ __attribute__((aligned(16))) char smem[];
  const int tid = threadIdx.x, lane = tid & 63;
  const int w = __builtin_amdgcn_readfirstlane(tid >> 6);
  const int T = (blockIdx.x & 7) * 32 + (blockIdx.x >> 3);
  if (T >= 252) return;
  const int p = T / 126, rem = T % 126, mg = rem / 7, ng = rem % 7;
  const char* of = (const char*)Of + (size_t)p * OF_PER_P * 2 + ((size_t)(2 * mg) * 256 + w * 32) * 1024 + lane * 16;
  const char* wo = (const char*)(p ? woB : woA) + (size_t)(w * 512) * (DM * 2) + ng * 128;
  const int hi = lane >> 5, blk = (lane >> 4) & 1, q4 = (lane & 15) >> 2, pp = lane & 3;
  const unsigned lds_base = (unsigned)(size_t)(lds_b4_ptr)smem + w * K2_WAVE_LDS;
  const unsigned voff = (unsigned)((lane >> 3) * (DM * 2) + (((lane & 7) ^ (((lane >> 4) & 1) << 2)) << 4));

  bf16x8 ob[K2_NBUF][2][2];
  auto issue = [&](int ck) {
#pragma unroll
    for (int mt = 0; mt < 2; ++mt)
#pragma unroll
      for (int ks = 0; ks < 2; ++ks) ob[ck % K2_NBUF][mt][ks] = gload16_asm(of + ((size_t)mt * 256 + 2 * ck + ks) * 1024);
#pragma unroll
    for (int n = 0; n < 4; ++n)
      glds16(wo + (size_t)(32 * ck + 8 * n) * (DM * 2), voff, lds_base + (ck % K2_NBUF) * 4096 + n * 1024);
  };
  f32x16 acc[2][2];
#pragma unroll
  for (int a = 0; a < 2; ++a)
#pragma unroll
    for (int c = 0; c < 2; ++c) acc[a][c] = f32x16{};
  issue(0); issue(1); issue(2); issue(3);
#pragma unroll
  for (int ck = 0; ck < 16; ++ck) {
    if (ck + 4 < 16) { issue(ck + 4); WAITV(32); }
    else if (ck == 12) WAITV(24); else if (ck == 13) WAITV(16); else if (ck == 14) WAITV(8); else WAITV(0);
    {
      bf16x8 (&o)[2][2] = ob[ck % K2_NBUF];
      asm volatile("" : "+v"(o[0][0]), "+v"(o[0][1]), "+v"(o[1][0]), "+v"(o[1][1]));
    }
    const unsigned sb = w * K2_WAVE_LDS + (ck % K2_NBUF) * 4096;
    bf16x8 fa[2][2];
#pragma unroll
    for (int ks = 0; ks < 2; ++ks)
#pragma unroll
      for (int nt = 0; nt < 2; ++nt) {
        const int row0 = 16 * ks + 8 * hi + q4, row1 = row0 + 4;
        const int ch = 4 * nt + 2 * blk + (pp >> 1);
        fa[ks][nt] = tr_read2(smem, sb + row0 * 128 + ((ch ^ (((row0 >> 1) & 1) << 2)) << 4) + 8 * (pp & 1),
                              sb + row1 * 128 + ((ch ^ (((row1 >> 1) & 1) << 2)) << 4) + 8 * (pp & 1));
      }
#pragma unroll
    for (int ks = 0; ks < 2; ++ks)
#pragma unroll
      for (int nt = 0; nt < 2; ++nt)
#pragma unroll
        for (int mt = 0; mt < 2; ++mt) acc[nt][mt] = mfma32(fa[ks][nt], ob[ck % K2_NBUF][mt][ks], acc[nt][mt]);
    asm volatile("s_waitcnt lgkmcnt(0)" ::: "memory");
  }
  __syncthreads();
  const int r32 = lane & 31;
#pragma unroll
  for (int nt = 0; nt < 2; ++nt)
#pragma unroll
    for (int mt = 0; mt < 2; ++mt)
#pragma unroll
      for (int qd = 0; qd < 4; ++qd) {
        f32x4 v = {acc[nt][mt][4 * qd], acc[nt][mt][4 * qd + 1], acc[nt][mt][4 * qd + 2], acc[nt][mt][4 * qd + 3]};
        *reinterpret_cast<f32x4*>(smem + ((size_t)w * 64 + 32 * mt + r32) * 272 + (32 * nt + 8 * qd + 4 * hi) * 4) = v;
      }
  __syncthreads();
  {
    const int m = tid >> 3, nc = tid & 7;
    float o[8];
#pragma unroll
    for (int j = 0; j < 8; ++j) o[j] = 0.f;
#pragma unroll
    for (int ww = 0; ww < 8; ++ww) {
      const f32x4 a = *reinterpret_cast<const f32x4*>(smem + ((size_t)ww * 64 + m) * 272 + nc * 32);
      const f32x4 c = *reinterpret_cast<const f32x4*>(smem + ((size_t)ww * 64 + m) * 272 + nc * 32 + 16);
#pragma unroll
      for (int j = 0; j < 4; ++j) { o[j] += a[j]; o[4 + j] += c[j]; }
    }
    const int mt = 2 * mg + (m >> 5), r = m & 31;
    int row;
    if (mt < 32) row = (mt >> 2) * SQ + (mt & 3) * 32 + r;
    else row = (2 * (mt - 32) + (r >> 4)) * SQ + 128 + (r & 15);
    bf16x8 v = pack8(o[0], o[1], o[2], o[3], o[4], o[5], o[6], o[7]);
    *reinterpret_cast<bf16x8*>((char*)out + ((size_t)row * (2 * DM) + p * DM + ng * 64 + nc * 8) * 2) = v;
  }
}

extern "C" void kernel_launch(void* const* d_in, const int* in_sizes, int n_in, void* d_out, int out_size, void* d_ws,
                              size_t ws_size, hipStream_t stream) {
  const __hip_bfloat16* xA = (const __hip_bfloat16*)d_in[0];
  const __hip_bfloat16* kA = (const __hip_bfloat16*)d_in[1];
  const __hip_bfloat16* vA = (const __hip_bfloat16*)d_in[2];
  const __hip_bfloat16* wqA = (const __hip_bfloat16*)d_in[3];
  const __hip_bfloat16* woA = (const __hip_bfloat16*)d_in[4];
  const __hip_bfloat16* xB = (const __hip_bfloat16*)d_in[5];
  const __hip_bfloat16* kB = (const __hip_bfloat16*)d_in[6];
  const __hip_bfloat16* vB = (const __hip_bfloat16*)d_in[7];
  const __hip_bfloat16* wqB = (const __hip_bfloat16*)d_in[8];
  const __hip_bfloat16* woB = (const __hip_bfloat16*)d_in[9];
  __hip_bfloat16* Of = (__hip_bfloat16*)d_ws;
  hipFuncSetAttribute((const void*)attn_kernel, hipFuncAttributeMaxDynamicSharedMemorySize, LDS_BYTES);
  hipFuncSetAttribute((const void*)oproj_kernel, hipFuncAttributeMaxDynamicSharedMemorySize, 8 * K2_WAVE_LDS);
  attn_kernel<<<256, 256, LDS_BYTES, stream>>>(xA, kA, vA, wqA, xB, kB, vB, wqB, Of
  );
  oproj_kernel<<<256, 512, 8 * K2_WAVE_LDS, stream>>>(Of, woA, woB, (__hip_bfloat16*)d_out);
}
```

```cpp
#include <hip/hip_runtime.h>
#include <hip/hip_bf16.h>
#include <stdint.h>
#include <type_traits>
#define ILQ 2
#define ILF 4

typedef short bf16x8 __attribute__((ext_vector_type(8)));
typedef short bf16x4 __attribute__((ext_vector_type(4)));
typedef float f32x16 __attribute__((ext_vector_type(16)));
typedef float f32x4 __attribute__((ext_vector_type(4)));
typedef unsigned u32x4 __attribute__((ext_vector_type(4)));
typedef unsigned u32x2 __attribute__((ext_vector_type(2)));
typedef __bf16 bf2_t __attribute__((ext_vector_type(2)));
typedef __attribute__((address_space(3))) bf16x4* lds_b4_ptr;

#define DI __device__ __forceinline__

constexpr int NB = 8, SQ = 144, DM = 448, NH = 16, SK = 512, DH = 256;
constexpr int SLOT = 32768;
constexpr int LDS_BYTES = 5 * SLOT;
constexpr size_t OF_PER_P = (size_t)36 * 256 * 512;

DI unsigned pack2(float a, float b) {
  bf2_t v = {(__bf16)a, (__bf16)b};
  return __builtin_bit_cast(unsigned, v);
}
DI bf16x8 pack8(float a0, float a1, float a2, float a3, float a4, float a5, float a6, float a7) {
  u32x4 w = {pack2(a0, a1), pack2(a2, a3), pack2(a4, a5), pack2(a6, a7)};
  return __builtin_bit_cast(bf16x8, w);
}
DI f32x16 mfma32(bf16x8 a, bf16x8 b, f32x16 c) { return __builtin_amdgcn_mfma_f32_32x32x16_bf16(a, b, c, 0, 0, 0); }
DI f32x4 mfma16x32(bf16x8 a, bf16x8 b, f32x4 c) { return __builtin_amdgcn_mfma_f32_16x16x32_bf16(a, b, c, 0, 0, 0); }
DI f32x4 mfma16x16(bf16x4 a, bf16x4 b, f32x4 c) { return __builtin_amdgcn_mfma_f32_16x16x16bf16_1k(a, b, c, 0, 0, 0); }

#define WAITV(n) asm volatile("s_waitcnt vmcnt(%0)" ::"n"(n) : "memory")
#define SB() __builtin_amdgcn_sched_barrier(0)
#define STAMP(i)
#define BARRIER() do { asm volatile("s_waitcnt lgkmcnt(0)" ::: "memory"); __builtin_amdgcn_s_barrier(); asm volatile("" ::: "memory"); } while (0)

DI bf16x4 tr_read(const char* smem, unsigned off) {
  return __builtin_amdgcn_ds_read_tr16_b64_v4i16((lds_b4_ptr)(smem + off));
}
DI bf16x8 tr_read2(const char* smem, unsigned off0, unsigned off1) {
  bf16x4 lo = tr_read(smem, off0), hi = tr_read(smem, off1);
  return __builtin_shufflevector(lo, hi, 0, 1, 2, 3, 4, 5, 6, 7);
}
DI void glds16(const char* gbase, unsigned voff, unsigned ldst) {
  unsigned keep;
  asm volatile("s_mov_b32 %0, m0\n\ts_mov_b32 m0, %3\n\ts_nop 0\n\tglobal_load_lds_dwordx4 %1, %2\n\ts_mov_b32 m0, %0"
               : "=&s"(keep) : "v"(voff), "s"(gbase), "s"(ldst) : "memory");
}
DI void glds16nt(const char* gbase, unsigned voff, unsigned ldst) {
  unsigned keep;
  asm volatile("s_mov_b32 %0, m0\n\ts_mov_b32 m0, %3\n\ts_nop 0\n\tglobal_load_lds_dwordx4 %1, %2 nt\n\ts_mov_b32 m0, %0"
               : "=&s"(keep) : "v"(voff), "s"(gbase), "s"(ldst) : "memory");
}
DI bf16x8 lds128(const char* smem, unsigned off) { return *reinterpret_cast<const bf16x8*>(smem + off); }

typedef __attribute__((address_space(3))) bf16x8* lds_b8_ptr;
DI bf16x4 trA(unsigned addr) { return __builtin_amdgcn_ds_read_tr16_b64_v4i16(reinterpret_cast<lds_b4_ptr>(addr)); }
DI bf16x8 trA2(unsigned a0, unsigned a1) {
  bf16x4 lo = trA(a0), hi = trA(a1);
  return __builtin_shufflevector(lo, hi, 0, 1, 2, 3, 4, 5, 6, 7);
}
DI bf16x8 ldA(unsigned addr) { return *reinterpret_cast<lds_b8_ptr>(addr); }
DI void ascale(float& a, float s) {
  float tmp;
  asm volatile("v_accvgpr_read_b32 %1, %0\n\tv_mul_f32 %1, %1, %2\n\tv_accvgpr_write_b32 %0, %1" : "+a"(a), "=&v"(tmp) : "v"(s));
}
DI void amfma32(f32x16& c, bf16x8 a, bf16x8 b) { asm volatile("v_mfma_f32_32x32x16_bf16 %0, %1, %2, %0" : "+a"(c) : "v"(a), "v"(b)); }
DI void amfma32z(f32x16& c, bf16x8 a, bf16x8 b) { asm volatile("v_mfma_f32_32x32x16_bf16 %0, %1, %2, 0" : "=a"(c) : "v"(a), "v"(b)); }
DI void amfma16x32(f32x4& c, bf16x8 a, bf16x8 b) { asm volatile("v_mfma_f32_16x16x32_bf16 %0, %1, %2, %0" : "+a"(c) : "v"(a), "v"(b)); }
DI void amfma16x32z(f32x4& c, bf16x8 a, bf16x8 b) { asm volatile("v_mfma_f32_16x16x32_bf16 %0, %1, %2, 0" : "=a"(c) : "v"(a), "v"(b)); }
DI void amfma16x16(f32x4& c, bf16x4 a, bf16x4 b) { asm volatile("v_mfma_f32_16x16x16_bf16 %0, %1, %2, %0" : "+a"(c) : "v"(a), "v"(b)); }
#define MFMA_DRAIN() asm volatile("s_nop 15\n\ts_nop 15" ::: "memory")
#define OPAQUE(x) asm volatile("" : "+v"(x))

DI int swzT(int row) { return ((row & 3) << 2) | ((((row >> 2) ^ (row >> 3)) & 1) << 1); }
DI int swap2(int p) { return ((p & 1) << 1) | (p >> 1); }

constexpr int STG = 53248;

DI void M32(int A, bf16x8 a, bf16x8 b) { asm volatile("v_mfma_f32_32x32x16_bf16 a[%c2:%c3], %0, %1, a[%c2:%c3]" ::"v"(a), "v"(b), "i"(A), "i"(A + 15)); }
DI void M32Z(int A, bf16x8 a, bf16x8 b) { asm volatile("v_mfma_f32_32x32x16_bf16 a[%c2:%c3], %0, %1, 0" ::"v"(a), "v"(b), "i"(A), "i"(A + 15)); }
DI void M16x32(int A, bf16x8 a, bf16x8 b) { asm volatile("v_mfma_f32_16x16x32_bf16 a[%c2:%c3], %0, %1, a[%c2:%c3]" ::"v"(a), "v"(b), "i"(A), "i"(A + 3)); }
DI void M16x32Z(int A, bf16x8 a, bf16x8 b) { asm volatile("v_mfma_f32_16x16x32_bf16 a[%c2:%c3], %0, %1, 0" ::"v"(a), "v"(b), "i"(A), "i"(A + 3)); }
DI void M16x16(int A, bf16x4 a, bf16x4 b) { asm volatile("v_mfma_f32_16x16x16_bf16 a[%c2:%c3], %0, %1, a[%c2:%c3]" ::"v"(a), "v"(b), "i"(A), "i"(A + 3)); }
DI float AREAD(int A) { float r; asm volatile("v_accvgpr_read_b32 %0, a%c1" : "=v"(r) : "i"(A)); return r; }
DI void AZERO(int A) { asm volatile("v_accvgpr_write_b32 a%c0, 0" ::"i"(A)); }
DI void ASCALE(int A, float s) {
  float t;
  asm volatile("v_accvgpr_read_b32 %0, a%c2\n\tv_mul_f32 %0, %0, %1\n\tv_accvgpr_write_b32 a%c2, %0" : "=&v"(t) : "v"(s), "i"(A));
}
#define NOPS() asm volatile("s_nop 15\n\ts_nop 15")
DI void glds16p(const char* gbase, unsigned voff, unsigned ldst, int flag, bool nt) {
  unsigned keep;
  flag = __builtin_amdgcn_readfirstlane(flag);
  if (nt)
    asm volatile("s_cmp_eq_u32 %4, 0\n\ts_cbranch_scc1 1f\n\ts_mov_b32 %0, m0\n\ts_mov_b32 m0, %3\n\ts_nop 0\n\tglobal_load_lds_dwordx4 %1, %2 nt\n\ts_mov_b32 m0, %0\n1:"
                 : "=&s"(keep) : "v"(voff), "s"(gbase), "s"(ldst), "s"(flag) : "memory", "scc");
  else
    asm volatile("s_cmp_eq_u32 %4, 0\n\ts_cbranch_scc1 1f\n\ts_mov_b32 %0, m0\n\ts_mov_b32 m0, %3\n\ts_nop 0\n\tglobal_load_lds_dwordx4 %1, %2\n\ts_mov_b32 m0, %0\n1:"
                 : "=&s"(keep) : "v"(voff), "s"(gbase), "s"(ldst), "s"(flag) : "memory", "scc");
}

__global__ __launch_bounds__(256, 1) void attn_kernel(
    const __hip_bfloat16* __restrict__ xA, const __hip_bfloat16* __restrict__ kA, const __hip_bfloat16* __restrict__ vA,
    const __hip_bfloat16* __restrict__ wqA, const __hip_bfloat16* __restrict__ xB, const __hip_bfloat16* __restrict__ kB,
    const __hip_bfloat16* __restrict__ vB, const __hip_bfloat16* __restrict__ wqB, __hip_bfloat16* __restrict__ Of
    ) {
  extern __shared__ __attribute__((aligned(16))) char smem[];
  asm volatile("" ::: "a0", "a64", "a128", "a192", "a227");
  const int tid = threadIdx.x, lane = tid & 63;
  const int w = __builtin_amdgcn_readfirstlane(tid >> 6);
  const int bid = blockIdx.x, xcd = bid & 7, idx = bid >> 3;
  const int ph = xcd * 4 + (idx >> 3), p = ph >> 4, h = ph & 15, b = idx & 7;
  const char* xg = (const char*)(p ? xB : xA) + (size_t)b * SQ * DM * 2;
  const char* wg = (const char*)(p ? wqB : wqA) + (size_t)h * DH * 2;
  const char* kg = (const char*)(p ? kB : kA) + (size_t)(b * NH + h) * SK * DH * 2;
  const char* vg = (const char*)(p ? vB : vA) + (size_t)(b * NH + h) * SK * DH * 2;

  int r32 = lane & 31, hi = lane >> 5, i16 = lane & 15, g = lane >> 4;
  int blk = (lane >> 4) & 1, q4 = (lane & 15) >> 2, pp = lane & 3;
#define RELANE() do { int l_ = lane; asm volatile("" : "+v"(l_)); r32 = l_ & 31; hi = l_ >> 5; i16 = l_ & 15; g = l_ >> 4; \
    blk = (l_ >> 4) & 1; q4 = (l_ & 15) >> 2; pp = l_ & 3; } while (0)

  const unsigned lds_base = (unsigned)(size_t)(lds_b4_ptr)smem;
  const unsigned voffK = (unsigned)((16 * w + hi) * 512 + ((r32 ^ hi) << 4));
  const unsigned voffV = (unsigned)((16 * w + hi) * 512 + ((r32 ^ (hi << 2)) << 4));
  const unsigned voffW = (unsigned)((16 * w + hi) * 8192 + ((r32 ^ (hi << 2)) << 4));
  unsigned voffX[5];
#pragma unroll
  for (int n = 0; n < 5; ++n) {
    const int gi = 5 * w + n;
    int row = 8 * gi + (lane >> 3);
    const int ch = (lane & 7) ^ ((row >> 1) & 7);
    row = row < SQ ? row : SQ - 1;
    voffX[n] = (unsigned)(row * (DM * 2) + ch * 16);
  }
  auto pieceKV = [&](const char* gb, bool isK, unsigned ldst, int n, int flag) {
    const unsigned x = isK ? (unsigned)(2 * n) : (unsigned)((((2 * n) & 3) << 2) | ((((n >> 1) ^ (n >> 2)) & 1) << 1));
    unsigned vb = isK ? voffK : voffV; OPAQUE(vb);
    glds16p(gb + n * 1024, vb ^ (x << 4), lds_base + ldst + w * 8192 + n * 1024, flag, true);
  };
  auto pieceQ = [&](int c, unsigned ldst, int n, int flag) {
    if (n < 8) {
      const unsigned x = (unsigned)((((2 * n) & 3) << 2) | ((((n >> 1) ^ (n >> 2)) & 1) << 1));
      unsigned vb = voffW; OPAQUE(vb);
      glds16p(wg + (size_t)c * (64 * NH * DH * 2) + n * 16384, vb ^ (x << 4), lds_base + ldst + w * 8192 + n * 1024, flag, false);
    } else {
      glds16p(xg + c * 128, voffX[n - 8], lds_base + ldst + SLOT + (5 * w + n - 8) * 1024, flag, false);
    }
  };
  STAMP(0);
#pragma unroll
  for (int n = 0; n < 13; ++n) pieceQ(0, 0, n, 1);
#pragma unroll
  for (int n = 0; n < 13; ++n) pieceQ(1, STG, n, 1);
#pragma unroll
  for (int i = 0; i < 192; ++i) AZERO(i);

  {
    const int spw = swap2(pp);
    const unsigned laneW = (unsigned)((8 * hi + q4) * 512 + ((((q4 << 2) | ((blk ^ hi) << 1)) ^ (spw >> 1)) << 4) + 8 * (spw & 1));
    const unsigned lane9 = (unsigned)((8 * g + q4) * 512 + (((8 * w + (pp >> 1)) ^ ((q4 << 2) | ((g & 1) << 1))) << 4) + 8 * (pp & 1));
    const unsigned laneX = (unsigned)(SLOT + (32 * w + r32) * 128 + ((hi ^ ((r32 >> 1) & 7)) << 4));
    const unsigned laneX9 = (unsigned)(SLOT + (128 + i16) * 128 + ((g ^ ((i16 >> 1) & 7)) << 4));
    unsigned stg = 0;
#pragma unroll 1
    for (int c = 0; c < 7; ++c) {
      STAMP(1 + 2 * c);
      if (c < 6) WAITV(13); else WAITV(8);
      BARRIER();
      STAMP(2 + 2 * c);
      bf16x8 fr[8], xq[4], x9q[2];
      unsigned bW0 = lds_base + stg + laneW; OPAQUE(bW0);
      unsigned bX = lds_base + stg + laneX; OPAQUE(bX);
      unsigned bX9 = lds_base + stg + laneX9; OPAQUE(bX9);
      const unsigned bW1 = (bW0 ^ 32) + 2048;
      unsigned b9 = lds_base + stg + lane9; OPAQUE(b9);
      auto ldf = [&](int k) -> bf16x8 {
        if (k < 32) {
          const int ks = k >> 3, d = k & 7;
          return trA2((bW0 ^ ((d & 3) << 6)) + (d >> 2) * 256 + ks * 8192, (bW1 ^ ((d & 3) << 6)) + (d >> 2) * 256 + ks * 8192);
        } else {
          const int ks = (k - 32) >> 2, d = (k - 32) & 3;
          return trA2((b9 ^ (d << 5)) + ks * 16384, ((b9 ^ 32) ^ (d << 5)) + 2048 + ks * 16384);
        }
      };
#pragma unroll
      for (int ks = 0; ks < 4; ++ks) xq[ks] = ldA(bX ^ (ks << 5));
#pragma unroll
      for (int k = 0; k < 8; ++k) fr[k] = ldf(k);
#pragma unroll
      for (int ks = 0; ks < 2; ++ks) x9q[ks] = ldA(bX9 ^ (ks << 6));
      unsigned nx = stg + 2 * STG; nx = nx >= 3 * STG ? nx - 3 * STG : nx;
      const int fQ = c <= 4, f5 = c == 5, f6 = c == 6;
      const char* gA = f5 ? kg : kg + SLOT;
      const unsigned lA = f5 ? 2 * SLOT : 3 * SLOT;
      const int fA = f5 | f6;
      SB();
#pragma unroll
      for (int k = 0; k < 40; ++k) {
        if (k < 32) M32(16 * (k & 7), fr[k & 7], xq[k >> 3]);
        else M16x32(128 + 4 * ((k - 32) & 3), fr[k & 7], x9q[(k - 32) >> 2]);
        if (k + 8 < 40) fr[k & 7] = ldf(k + 8);
        if ((k % 3) == 0 && k / 3 < 13) pieceQ(c + 2, nx, k / 3, fQ);
        if ((k & 1) == 1 && k < 16) pieceKV(gA, true, lA, k >> 1, fA);
        if ((k & 1) == 1 && k >= 16 && k < 32) pieceKV(vg, false, 4 * SLOT, (k - 16) >> 1, f6);
        SB();
      }
      stg += STG; stg = stg >= 3 * STG ? 0 : stg;
    }
  }
  NOPS();
  bf16x8 Qf[16];
#pragma unroll
  for (int s = 0; s < 16; ++s) {
    const int A = 16 * (s >> 1) + 8 * (s & 1);
    Qf[s] = pack8(AREAD(A), AREAD(A + 1), AREAD(A + 2), AREAD(A + 3), AREAD(A + 4), AREAD(A + 5), AREAD(A + 6), AREAD(A + 7));
  }
  RELANE();
  BARRIER();
#pragma unroll
  for (int n = 0; n < 8; ++n) pieceKV(kg + 2 * SLOT, true, 0, n, 1);
#pragma unroll
  for (int d = 0; d < 4; ++d) {
    u32x2 v = {pack2(AREAD(128 + 4 * d), AREAD(129 + 4 * d)), pack2(AREAD(130 + 4 * d), AREAD(131 + 4 * d))};
    const int dh = 64 * w + 16 * d + 4 * g;
    *reinterpret_cast<u32x2*>(smem + SLOT + i16 * 512 + (((dh >> 3) ^ i16) << 4) + ((dh & 4) << 1)) = v;
  }
#pragma unroll
  for (int i = 0; i < 192; ++i) AZERO(i);
  bf16x8 Q9f[8];
  BARRIER();
#pragma unroll
  for (int s = 0; s < 8; ++s) Q9f[s] = ldA(lds_base + SLOT + i16 * 512 + (((4 * s + g) ^ i16) << 4));

  constexpr float CS = 0.0625f * 1.4426950408889634f;
  constexpr float THR = 8.0f / CS;
  float m = -3.0e38f, l = 0.f, m9 = -3.0e38f, l9 = 0.f;
  const unsigned laneK = (unsigned)(r32 * 512 + ((hi ^ (r32 & 15)) << 4));
  const unsigned laneK9 = (unsigned)((16 * w + i16) * 512 + ((g ^ i16) << 4));
  const int spv = swap2(pp);
  const unsigned laneV = (unsigned)((4 * hi + q4) * 512 + ((((q4 << 2) | ((blk ^ hi) << 1)) ^ (spv >> 1)) << 4) + 8 * (spv & 1));
  const unsigned laneV9 = (unsigned)((16 * w + 4 * g + q4) * 512 + (((pp >> 1) ^ ((q4 << 2) | (((g ^ (g >> 1)) & 1) << 1))) << 4) + 8 * (pp & 1));
  int sl = 2;
  float sv[36];
  float mt, mt9;
  bf16x8 pf[4];
  bf16x4 p9f;

  auto s_phase = [&](auto&& extra) {
    bf16x8 fr[8];
    unsigned bK = lds_base + sl * SLOT + laneK; OPAQUE(bK);
    unsigned bK9 = lds_base + sl * SLOT + laneK9; OPAQUE(bK9);
    auto ldf = [&](int k) -> bf16x8 {
      if (k < 32) return ldA((bK ^ ((k & 7) << 5)) + ((k >> 3) & 1) * 256 + (k >> 4) * 16384);
      else return ldA((bK9 ^ ((k & 3) << 6)) + ((k >> 2) & 1) * 256);
    };
#pragma unroll
    for (int k = 0; k < 8; ++k) fr[k] = ldf(k);
    SB();
#pragma unroll
    for (int k = 0; k < 40; ++k) {
      if (k < 32) { if ((k & 15) == 0) M32Z(192 + 16 * (k >> 4), fr[k & 7], Qf[k & 15]); else M32(192 + 16 * (k >> 4), fr[k & 7], Qf[k & 15]); }
      else { if (k == 32) M16x32Z(224, fr[k & 7], Q9f[k - 32]); else M16x32(224, fr[k & 7], Q9f[k - 32]); }
      if (k + 8 < 40) fr[k & 7] = ldf(k + 8);
      extra(k);
      SB();
    }
    sl = sl == 4 ? 0 : sl + 1;
  };
  auto read_scores = [&](int k) {
    if (k < 9) {
#pragma unroll
      for (int i = 0; i < 4; ++i) sv[4 * k + i] = AREAD(192 + 4 * k + i);
    }
    if (k >= 1 && k < 9) {
      const int j = 4 * (k - 1);
      const float v = fmaxf(fmaxf(sv[j], sv[j + 1]), fmaxf(sv[j + 2], sv[j + 3]));
      mt = k == 1 ? v : fmaxf(mt, v);
    }
    if (k == 9) {
      mt9 = fmaxf(fmaxf(sv[32], sv[33]), fmaxf(sv[34], sv[35]));
      mt = fmaxf(mt, __shfl_xor(mt, 32));
      mt9 = fmaxf(mt9, __shfl_xor(mt9, 16));
    }
    if (k == 14) mt9 = fmaxf(mt9, __shfl_xor(mt9, 32));
  };

  WAITV(24);
  BARRIER();
  s_phase([&](int k) { if ((k & 3) == 1 && (k >> 2) < 8) pieceKV(vg + SLOT, false, 1 * SLOT, k >> 2, 1); });
  NOPS();
#pragma unroll
  for (int k = 0; k < 15; ++k) read_scores(k);

#pragma unroll 1
  for (int t = 0; t < 8; ++t) {
    STAMP(15 + 2 * (t));
    if (t < 6) WAITV(24); else if (t == 6) WAITV(16);
    BARRIER();
    STAMP(16 + 2 * (t));
    if (__any((mt - m > THR) || (mt9 - m9 > THR))) {
      const float mn = fmaxf(m, mt), mn9 = fmaxf(m9, mt9);
      const float al = __builtin_amdgcn_exp2f((m - mn) * CS), al9 = __builtin_amdgcn_exp2f((m9 - mn9) * CS);
      m = mn; m9 = mn9; l *= al; l9 *= al9;
#pragma unroll
      for (int i = 0; i < 128; ++i) ASCALE(i, al);
#pragma unroll
      for (int i = 128; i < 192; ++i) ASCALE(i, al9);
    }
    float mc = m * CS, mc9 = m9 * CS;
    unsigned pk[18];
    auto sm = [&](int pr) {
      const float c_ = pr < 16 ? mc : mc9;
      const float e0 = __builtin_amdgcn_exp2f(fmaf(sv[2 * pr], CS, -c_)), e1 = __builtin_amdgcn_exp2f(fmaf(sv[2 * pr + 1], CS, -c_));
      if (pr < 16) l += e0 + e1; else l9 += e0 + e1;
      pk[pr] = pack2(e0, e1);
    };
    if (t < 7) {
      const int fl = t < 5;
      const int ps = sl == 0 ? 4 : sl - 1;
      OPAQUE(mc); OPAQUE(mc9);
      s_phase([&](int k) {
        if ((k & 1) == 0 && (k >> 1) < 18) sm(k >> 1);
        if ((k & 3) == 1 && (k >> 2) < 8) pieceKV(kg + (size_t)(t + 3) * SLOT, true, ps * SLOT, k >> 2, fl);
      });
    } else {
      OPAQUE(mc); OPAQUE(mc9);
#pragma unroll
      for (int pr = 0; pr < 18; ++pr) sm(pr);
      sl = sl == 4 ? 0 : sl + 1;
    }
#pragma unroll
    for (int i = 0; i < 4; ++i) {
      u32x4 wv = {pk[4 * i], pk[4 * i + 1], pk[4 * i + 2], pk[4 * i + 3]};
      pf[i] = __builtin_bit_cast(bf16x8, wv);
    }
    {
      u32x2 wv = {pk[16], pk[17]};
      p9f = __builtin_bit_cast(bf16x4, wv);
    }
    STAMP(31 + 2 * (t));
    if (t < 5) WAITV(24); else if (t == 5) WAITV(16); else if (t == 6) WAITV(8); else WAITV(0);
    BARRIER();
    STAMP(32 + 2 * (t));
    {
      bf16x8 fr[8];
      unsigned bV0 = lds_base + sl * SLOT + laneV; OPAQUE(bV0);
      const unsigned bV1 = (bV0 ^ 32) + 4096;
      unsigned bV9 = lds_base + sl * SLOT + laneV9; OPAQUE(bV9);
      auto ldf = [&](int k) -> bf16x8 {
        if (k < 32) {
          const int kp = k >> 3, d = k & 7;
          return trA2((bV0 ^ ((d & 3) << 6)) + (d >> 2) * 256 + kp * 8192, (bV1 ^ ((d & 3) << 6)) + (d >> 2) * 256 + kp * 8192);
        } else {
          const int d = k - 32;
          return trA2((bV9 ^ ((d & 3) << 6)) + (d >> 2) * 256, ((bV9 ^ 32) ^ ((d & 3) << 6)) + (d >> 2) * 256);
        }
      };
#pragma unroll
      for (int k = 0; k < 8; ++k) fr[k] = ldf(k);
      const int fl = t < 6;
      const int ps = sl == 0 ? 4 : sl - 1;
      SB();
#pragma unroll
      for (int k = 0; k < 40; ++k) {
        if (k < 32) M32(16 * (k & 7), fr[k & 7], pf[k >> 3]);
        else {
          const int d = k - 32;
          bf16x4 lo = __builtin_shufflevector(fr[k & 7], fr[k & 7], 0, 1, 2, 3), hi4 = __builtin_shufflevector(fr[k & 7], fr[k & 7], 4, 5, 6, 7);
          M16x16(128 + 8 * d, lo, p9f);
          M16x16(132 + 8 * d, hi4, p9f);
        }
        if (k + 8 < 40) fr[k & 7] = ldf(k + 8);
        if ((k & 3) == 1 && (k >> 2) < 8) pieceKV(vg + (size_t)(t + 2) * SLOT, false, ps * SLOT, k >> 2, fl);
        if (k >= 2 && k < 17) read_scores(k - 2);
        SB();
      }
      sl = sl == 4 ? 0 : sl + 1;
    }
  }
  NOPS();
  l += __shfl_xor(l, 32);
  l9 += __shfl_xor(l9, 16);
  l9 += __shfl_xor(l9, 32);
  const float inv_l = 1.0f / l;

  STAMP(47);
  char* ofp = (char*)Of + (size_t)p * OF_PER_P * 2;
  {
    char* dst = ofp + ((size_t)(4 * b + w) * 256 + h * 16) * 1024 + lane * 16;
#pragma unroll
    for (int s = 0; s < 16; ++s) {
      const int A = 16 * (s >> 1) + 8 * (s & 1);
      bf16x8 v = pack8(AREAD(A) * inv_l, AREAD(A + 1) * inv_l, AREAD(A + 2) * inv_l, AREAD(A + 3) * inv_l,
                       AREAD(A + 4) * inv_l, AREAD(A + 5) * inv_l, AREAD(A + 6) * inv_l, AREAD(A + 7) * inv_l);
      *reinterpret_cast<bf16x8*>(dst + s * 1024) = v;
    }
  }
  RELANE();
  BARRIER();
  {
    float* st = reinterpret_cast<float*>(smem + 4 * 16 * 1040);
#pragma unroll
    for (int d = 0; d < 16; ++d) {
      f32x4 v = {AREAD(128 + 4 * d), AREAD(129 + 4 * d), AREAD(130 + 4 * d), AREAD(131 + 4 * d)};
      *reinterpret_cast<f32x4*>(smem + (w * 16 + i16) * 1040 + (16 * d + 4 * g) * 4) = v;
    }
    if (g == 0) { st[(w * 16 + i16) * 2] = m9 * CS; st[(w * 16 + i16) * 2 + 1] = l9; }
    BARRIER();
#pragma unroll
    for (int it = 0; it < 2; ++it) {
      const int item = tid + 256 * it;
      const int q = item >> 5, ck = item & 31;
      float mw[4], lw[4];
#pragma unroll
      for (int ww = 0; ww < 4; ++ww) { mw[ww] = st[(ww * 16 + q) * 2]; lw[ww] = st[(ww * 16 + q) * 2 + 1]; }
      const float M = fmaxf(fmaxf(mw[0], mw[1]), fmaxf(mw[2], mw[3]));
      float f[4], L = 0.f;
#pragma unroll
      for (int ww = 0; ww < 4; ++ww) { f[ww] = __builtin_amdgcn_exp2f(mw[ww] - M); L += f[ww] * lw[ww]; }
      const float il = 1.0f / L;
      float o[8];
#pragma unroll
      for (int j = 0; j < 8; ++j) o[j] = 0.f;
#pragma unroll
      for (int ww = 0; ww < 4; ++ww) {
        const f32x4 a = *reinterpret_cast<const f32x4*>(smem + (ww * 16 + q) * 1040 + ck * 32);
        const f32x4 c2 = *reinterpret_cast<const f32x4*>(smem + (ww * 16 + q) * 1040 + ck * 32 + 16);
#pragma unroll
        for (int j = 0; j < 4; ++j) { o[j] += f[ww] * a[j]; o[4 + j] += f[ww] * c2[j]; }
      }
      bf16x8 v = pack8(o[0] * il, o[1] * il, o[2] * il, o[3] * il, o[4] * il, o[5] * il, o[6] * il, o[7] * il);
      char* dst = ofp + ((size_t)(32 + (b >> 1)) * 256 + h * 16 + (ck >> 1)) * 1024 + (16 * (b & 1) + q + 32 * (ck & 1)) * 16;
      *reinterpret_cast<bf16x8*>(dst) = v;
    }
  }
  STAMP(48);
}

constexpr int K2_NBUF = 5;
constexpr int K2_WAVE_LDS = K2_NBUF * 4096;
DI bf16x8 gload16_asm(const char* p) {
  bf16x8 r;
  asm volatile("global_load_dwordx4 %0, %1, off" : "=v"(r) : "v"(p) : "memory");
  return r;
}
__global__ __launch_bounds__(512, 2) void oproj_kernel(const __hip_bfloat16* __restrict__ Of, const __hip_bfloat16* __restrict__ woA,
                                                       const __hip_bfloat16* __restrict__ woB, __hip_bfloat16* __restrict__ out) {
  extern __shared__ __attribute__((aligned(16))) char smem[];
  const int tid = threadIdx.x, lane = tid & 63;
  const int w = __builtin_amdgcn_readfirstlane(tid >> 6);
  const int T = (blockIdx.x & 7) * 32 + (blockIdx.x >> 3);
  if (T >= 252) return;
  const int p = T / 126, rem = T % 126, mg = rem / 7, ng = rem % 7;
  const char* of = (const char*)Of + (size_t)p * OF_PER_P * 2 + ((size_t)(2 * mg) * 256 + w * 32) * 1024 + lane * 16;
  const char* wo = (const char*)(p ? woB : woA) + (size_t)(w * 512) * (DM * 2) + ng * 128;
  const int hi = lane >> 5, blk = (lane >> 4) & 1, q4 = (lane & 15) >> 2, pp = lane & 3;
  const unsigned lds_base = (unsigned)(size_t)(lds_b4_ptr)smem + w * K2_WAVE_LDS;
  const unsigned voff = (unsigned)((lane >> 3) * (DM * 2) + (((lane & 7) ^ (((lane >> 4) & 1) << 2)) << 4));

  bf16x8 ob[K2_NBUF][2][2];
  auto issue = [&](int ck) {
#pragma unroll
    for (int mt = 0; mt < 2; ++mt)
#pragma unroll
      for (int ks = 0; ks < 2; ++ks) ob[ck % K2_NBUF][mt][ks] = gload16_asm(of + ((size_t)mt * 256 + 2 * ck + ks) * 1024);
#pragma unroll
    for (int n = 0; n < 4; ++n)
      glds16(wo + (size_t)(32 * ck + 8 * n) * (DM * 2), voff, lds_base + (ck % K2_NBUF) * 4096 + n * 1024);
  };
  f32x16 acc[2][2];
#pragma unroll
  for (int a = 0; a < 2; ++a)
#pragma unroll
    for (int c = 0; c < 2; ++c) acc[a][c] = f32x16{};
  issue(0); issue(1); issue(2); issue(3);
#pragma unroll
  for (int ck = 0; ck < 16; ++ck) {
    if (ck + 4 < 16) { issue(ck + 4); WAITV(32); }
    else if (ck == 12) WAITV(24); else if (ck == 13) WAITV(16); else if (ck == 14) WAITV(8); else WAITV(0);
    {
      bf16x8 (&o)[2][2] = ob[ck % K2_NBUF];
      asm volatile("" : "+v"(o[0][0]), "+v"(o[0][1]), "+v"(o[1][0]), "+v"(o[1][1]));
    }
    const unsigned sb = w * K2_WAVE_LDS + (ck % K2_NBUF) * 4096;
    bf16x8 fa[2][2];
#pragma unroll
    for (int ks = 0; ks < 2; ++ks)
#pragma unroll
      for (int nt = 0; nt < 2; ++nt) {
        const int row0 = 16 * ks + 8 * hi + q4, row1 = row0 + 4;
        const int ch = 4 * nt + 2 * blk + (pp >> 1);
        fa[ks][nt] = tr_read2(smem, sb + row0 * 128 + ((ch ^ (((row0 >> 1) & 1) << 2)) << 4) + 8 * (pp & 1),
                              sb + row1 * 128 + ((ch ^ (((row1 >> 1) & 1) << 2)) << 4) + 8 * (pp & 1));
      }
#pragma unroll
    for (int ks = 0; ks < 2; ++ks)
#pragma unroll
      for (int nt = 0; nt < 2; ++nt)
#pragma unroll
        for (int mt = 0; mt < 2; ++mt) acc[nt][mt] = mfma32(fa[ks][nt], ob[ck % K2_NBUF][mt][ks], acc[nt][mt]);
    asm volatile("s_waitcnt lgkmcnt(0)" ::: "memory");
  }
  __syncthreads();
  const int r32 = lane & 31;
#pragma unroll
  for (int nt = 0; nt < 2; ++nt)
#pragma unroll
    for (int mt = 0; mt < 2; ++mt)
#pragma unroll
      for (int qd = 0; qd < 4; ++qd) {
        f32x4 v = {acc[nt][mt][4 * qd], acc[nt][mt][4 * qd + 1], acc[nt][mt][4 * qd + 2], acc[nt][mt][4 * qd + 3]};
        *reinterpret_cast<f32x4*>(smem + ((size_t)w * 64 + 32 * mt + r32) * 272 + (32 * nt + 8 * qd + 4 * hi) * 4) = v;
      }
  __syncthreads();
  {
    const int m = tid >> 3, nc = tid & 7;
    float o[8];
#pragma unroll
    for (int j = 0; j < 8; ++j) o[j] = 0.f;
#pragma unroll
    for (int ww = 0; ww < 8; ++ww) {
      const f32x4 a = *reinterpret_cast<const f32x4*>(smem + ((size_t)ww * 64 + m) * 272 + nc * 32);
      const f32x4 c = *reinterpret_cast<const f32x4*>(smem + ((size_t)ww * 64 + m) * 272 + nc * 32 + 16);
#pragma unroll
      for (int j = 0; j < 4; ++j) { o[j] += a[j]; o[4 + j] += c[j]; }
    }
    const int mt = 2 * mg + (m >> 5), r = m & 31;
    int row;
    if (mt < 32) row = (mt >> 2) * SQ + (mt & 3) * 32 + r;
    else row = (2 * (mt - 32) + (r >> 4)) * SQ + 128 + (r & 15);
    bf16x8 v = pack8(o[0], o[1], o[2], o[3], o[4], o[5], o[6], o[7]);
    *reinterpret_cast<bf16x8*>((char*)out + ((size_t)row * (2 * DM) + p * DM + ng * 64 + nc * 8) * 2) = v;
  }
}

extern "C" void kernel_launch(void* const* d_in, const int* in_sizes, int n_in, void* d_out, int out_size, void* d_ws,
                              size_t ws_size, hipStream_t stream) {
  const __hip_bfloat16* xA = (const __hip_bfloat16*)d_in[0];
  const __hip_bfloat16* kA = (const __hip_bfloat16*)d_in[1];
  const __hip_bfloat16* vA = (const __hip_bfloat16*)d_in[2];
  const __hip_bfloat16* wqA = (const __hip_bfloat16*)d_in[3];
  const __hip_bfloat16* woA = (const __hip_bfloat16*)d_in[4];
  const __hip_bfloat16* xB = (const __hip_bfloat16*)d_in[5];
  const __hip_bfloat16* kB = (const __hip_bfloat16*)d_in[6];
  const __hip_bfloat16* vB = (const __hip_bfloat16*)d_in[7];
  const __hip_bfloat16* wqB = (const __hip_bfloat16*)d_in[8];
  const __hip_bfloat16* woB = (const __hip_bfloat16*)d_in[9];
  __hip_bfloat16* Of = (__hip_bfloat16*)d_ws;
  hipFuncSetAttribute((const void*)attn_kernel, hipFuncAttributeMaxDynamicSharedMemorySize, LDS_BYTES);
  hipFuncSetAttribute((const void*)oproj_kernel, hipFuncAttributeMaxDynamicSharedMemorySize, 8 * K2_WAVE_LDS);
  attn_kernel<<<256, 256, LDS_BYTES, stream>>>(xA, kA, vA, wqA, xB, kB, vB, wqB, Of
  );
  oproj_kernel<<<256, 512, 8 * K2_WAVE_LDS, stream>>>(Of, woA, woB, (__hip_bfloat16*)d_out);
}
```

```cpp
#include <hip/hip_runtime.h>
#include <hip/hip_bf16.h>
#include <stdint.h>
#include <type_traits>
#define ILQ 2
#define ILF 4

typedef short bf16x8 __attribute__((ext_vector_type(8)));
typedef short bf16x4 __attribute__((ext_vector_type(4)));
typedef float f32x16 __attribute__((ext_vector_type(16)));
typedef float f32x4 __attribute__((ext_vector_type(4)));
typedef unsigned u32x4 __attribute__((ext_vector_type(4)));
typedef unsigned u32x2 __attribute__((ext_vector_type(2)));
typedef __bf16 bf2_t __attribute__((ext_vector_type(2)));
typedef __attribute__((address_space(3))) bf16x4* lds_b4_ptr;

#define DI __device__ __forceinline__

constexpr int NB = 8, SQ = 144, DM = 448, NH = 16, SK = 512, DH = 256;
constexpr int SLOT = 32768;
constexpr int LDS_BYTES = 5 * SLOT;
constexpr size_t OF_PER_P = (size_t)36 * 256 * 512;

DI unsigned pack2(float a, float b) {
  bf2_t v = {(__bf16)a, (__bf16)b};
  return __builtin_bit_cast(unsigned, v);
}
DI bf16x8 pack8(float a0, float a1, float a2, float a3, float a4, float a5, float a6, float a7) {
  u32x4 w = {pack2(a0, a1), pack2(a2, a3), pack2(a4, a5), pack2(a6, a7)};
  return __builtin_bit_cast(bf16x8, w);
}
DI f32x16 mfma32(bf16x8 a, bf16x8 b, f32x16 c) { return __builtin_amdgcn_mfma_f32_32x32x16_bf16(a, b, c, 0, 0, 0); }
DI f32x4 mfma16x32(bf16x8 a, bf16x8 b, f32x4 c) { return __builtin_amdgcn_mfma_f32_16x16x32_bf16(a, b, c, 0, 0, 0); }
DI f32x4 mfma16x16(bf16x4 a, bf16x4 b, f32x4 c) { return __builtin_amdgcn_mfma_f32_16x16x16bf16_1k(a, b, c, 0, 0, 0); }

#define WAITV(n) asm volatile("s_waitcnt vmcnt(%0)" ::"n"(n) : "memory")
#define SB() __builtin_amdgcn_sched_barrier(0)
#define STAMP(i)
#define BARRIER() do { asm volatile("s_waitcnt lgkmcnt(0)" ::: "memory"); __builtin_amdgcn_s_barrier(); asm volatile("" ::: "memory"); } while (0)

DI bf16x4 tr_read(const char* smem, unsigned off) {
  return __builtin_amdgcn_ds_read_tr16_b64_v4i16((lds_b4_ptr)(smem + off));
}
DI bf16x8 tr_read2(const char* smem, unsigned off0, unsigned off1) {
  bf16x4 lo = tr_read(smem, off0), hi = tr_read(smem, off1);
  return __builtin_shufflevector(lo, hi, 0, 1, 2, 3, 4, 5, 6, 7);
}
DI void glds16(const char* gbase, unsigned voff, unsigned ldst) {
  unsigned keep;
  asm volatile("s_mov_b32 %0, m0\n\ts_mov_b32 m0, %3\n\ts_nop 0\n\tglobal_load_lds_dwordx4 %1, %2\n\ts_mov_b32 m0, %0"
               : "=&s"(keep) : "v"(voff), "s"(gbase), "s"(ldst) : "memory");
}
DI void glds16nt(const char* gbase, unsigned voff, unsigned ldst) {
  unsigned keep;
  asm volatile("s_mov_b32 %0, m0\n\ts_mov_b32 m0, %3\n\ts_nop 0\n\tglobal_load_lds_dwordx4 %1, %2 nt\n\ts_mov_b32 m0, %0"
               : "=&s"(keep) : "v"(voff), "s"(gbase), "s"(ldst) : "memory");
}
DI bf16x8 lds128(const char* smem, unsigned off) { return *reinterpret_cast<const bf16x8*>(smem + off); }

typedef __attribute__((address_space(3))) bf16x8* lds_b8_ptr;
DI bf16x4 trA(unsigned addr) { return __builtin_amdgcn_ds_read_tr16_b64_v4i16(reinterpret_cast<lds_b4_ptr>(addr)); }
DI bf16x8 trA2(unsigned a0, unsigned a1) {
  bf16x4 lo = trA(a0), hi = trA(a1);
  return __builtin_shufflevector(lo, hi, 0, 1, 2, 3, 4, 5, 6, 7);
}
DI bf16x8 ldA(unsigned addr) { return *reinterpret_cast<lds_b8_ptr>(addr); }
DI void ascale(float& a, float s) {
  float tmp;
  asm volatile("v_accvgpr_read_b32 %1, %0\n\tv_mul_f32 %1, %1, %2\n\tv_accvgpr_write_b32 %0, %1" : "+a"(a), "=&v"(tmp) : "v"(s));
}
DI void amfma32(f32x16& c, bf16x8 a, bf16x8 b) { asm volatile("v_mfma_f32_32x32x16_bf16 %0, %1, %2, %0" : "+a"(c) : "v"(a), "v"(b)); }
DI void amfma32z(f32x16& c, bf16x8 a, bf16x8 b) { asm volatile("v_mfma_f32_32x32x16_bf16 %0, %1, %2, 0" : "=a"(c) : "v"(a), "v"(b)); }
DI void amfma16x32(f32x4& c, bf16x8 a, bf16x8 b) { asm volatile("v_mfma_f32_16x16x32_bf16 %0, %1, %2, %0" : "+a"(c) : "v"(a), "v"(b)); }
DI void amfma16x32z(f32x4& c, bf16x8 a, bf16x8 b) { asm volatile("v_mfma_f32_16x16x32_bf16 %0, %1, %2, 0" : "=a"(c) : "v"(a), "v"(b)); }
DI void amfma16x16(f32x4& c, bf16x4 a, bf16x4 b) { asm volatile("v_mfma_f32_16x16x16_bf16 %0, %1, %2, %0" : "+a"(c) : "v"(a), "v"(b)); }
#define MFMA_DRAIN() asm volatile("s_nop 15\n\ts_nop 15" ::: "memory")
#define OPAQUE(x) asm volatile("" : "+v"(x))

DI int swzT(int row) { return ((row & 3) << 2) | ((((row >> 2) ^ (row >> 3)) & 1) << 1); }
DI int swap2(int p) { return ((p & 1) << 1) | (p >> 1); }

constexpr int STG = 53248;

DI void M32(int A, bf16x8 a, bf16x8 b) { asm volatile("v_mfma_f32_32x32x16_bf16 a[%c2:%c3], %0, %1, a[%c2:%c3]" ::"v"(a), "v"(b), "i"(A), "i"(A + 15)); }
DI void M32Z(int A, bf16x8 a, bf16x8 b) { asm volatile("v_mfma_f32_32x32x16_bf16 a[%c2:%c3], %0, %1, 0" ::"v"(a), "v"(b), "i"(A), "i"(A + 15)); }
DI void M16x32(int A, bf16x8 a, bf16x8 b) { asm volatile("v_mfma_f32_16x16x32_bf16 a[%c2:%c3], %0, %1, a[%c2:%c3]" ::"v"(a), "v"(b), "i"(A), "i"(A + 3)); }
DI void M16x32Z(int A, bf16x8 a, bf16x8 b) { asm volatile("v_mfma_f32_16x16x32_bf16 a[%c2:%c3], %0, %1, 0" ::"v"(a), "v"(b), "i"(A), "i"(A + 3)); }
DI void M16x16(int A, bf16x4 a, bf16x4 b) { asm volatile("v_mfma_f32_16x16x16_bf16 a[%c2:%c3], %0, %1, a[%c2:%c3]" ::"v"(a), "v"(b), "i"(A), "i"(A + 3)); }
DI float AREAD(int A) { float r; asm volatile("v_accvgpr_read_b32 %0, a%c1" : "=v"(r) : "i"(A)); return r; }
DI void AZERO(int A) { asm volatile("v_accvgpr_write_b32 a%c0, 0" ::"i"(A)); }
DI void ASCALE(int A, float s) {
  float t;
  asm volatile("v_accvgpr_read_b32 %0, a%c2\n\tv_mul_f32 %0, %0, %1\n\tv_accvgpr_write_b32 a%c2, %0" : "=&v"(t) : "v"(s), "i"(A));
}
#define NOPS() asm volatile("s_nop 15\n\ts_nop 15")
template <int OFF, bool NT>
DI void dma(const char* gbase, unsigned voff, unsigned ldst) {
  if (NT) asm volatile("s_mov_b32 m0, %2\n\ts_nop 0\n\tglobal_load_lds_dwordx4 %0, %1 offset:%c3 nt" ::"v"(voff), "s"(gbase), "s"(ldst), "i"(OFF) : "memory");
  else asm volatile("s_mov_b32 m0, %2\n\ts_nop 0\n\tglobal_load_lds_dwordx4 %0, %1 offset:%c3" ::"v"(voff), "s"(gbase), "s"(ldst), "i"(OFF) : "memory");
}

__global__ __launch_bounds__(256, 1) void attn_kernel(
    const __hip_bfloat16* __restrict__ xA, const __hip_bfloat16* __restrict__ kA, const __hip_bfloat16* __restrict__ vA,
    const __hip_bfloat16* __restrict__ wqA, const __hip_bfloat16* __restrict__ xB, const __hip_bfloat16* __restrict__ kB,
    const __hip_bfloat16* __restrict__ vB, const __hip_bfloat16* __restrict__ wqB, __hip_bfloat16* __restrict__ Of
    ) {
  extern __shared__ __attribute__((aligned(16))) char smem[];
  asm volatile("" ::: "a0", "a64", "a128", "a192", "a227");
  const int tid = threadIdx.x, lane = tid & 63;
  const int w = __builtin_amdgcn_readfirstlane(tid >> 6);
  const int bid = blockIdx.x, xcd = bid & 7, idx = bid >> 3;
  const int ph = xcd * 4 + (idx >> 3), p = ph >> 4, h = ph & 15, b = idx & 7;
  const char* xg = (const char*)(p ? xB : xA) + (size_t)b * SQ * DM * 2;
  const char* wg = (const char*)(p ? wqB : wqA) + (size_t)h * DH * 2;
  const char* kg = (const char*)(p ? kB : kA) + (size_t)(b * NH + h) * SK * DH * 2;
  const char* vg = (const char*)(p ? vB : vA) + (size_t)(b * NH + h) * SK * DH * 2;

  int r32 = lane & 31, hi = lane >> 5, i16 = lane & 15, g = lane >> 4;
  int blk = (lane >> 4) & 1, q4 = (lane & 15) >> 2, pp = lane & 3;
#define RELANE() do { int l_ = lane; asm volatile("" : "+v"(l_)); r32 = l_ & 31; hi = l_ >> 5; i16 = l_ & 15; g = l_ >> 4; \
    blk = (l_ >> 4) & 1; q4 = (l_ & 15) >> 2; pp = l_ & 3; } while (0)

  const unsigned lds_base = (unsigned)(size_t)(lds_b4_ptr)smem;
  unsigned voffK[8], voffV[8], voffW[8], voffX[5];
#pragma unroll
  for (int n = 0; n < 8; ++n) {
    const unsigned xk = (unsigned)(2 * n), xv = (unsigned)((((2 * n) & 3) << 2) | ((((n >> 1) ^ (n >> 2)) & 1) << 1));
    voffK[n] = (unsigned)((16 * w + hi) * 512 + (((r32 ^ hi) ^ xk) << 4));
    voffV[n] = (unsigned)((16 * w + hi) * 512 + (((r32 ^ (hi << 2)) ^ xv) << 4));
    voffW[n] = (unsigned)((16 * w + hi + 2 * n) * 8192 + (((r32 ^ (hi << 2)) ^ xv) << 4));
  }
#pragma unroll
  for (int n = 0; n < 5; ++n) {
    const int gi = 5 * w + n;
    int row = 8 * gi + (lane >> 3);
    const int ch = (lane & 7) ^ ((row >> 1) & 7);
    row = row < SQ ? row : SQ - 1;
    voffX[n] = (unsigned)(row * (DM * 2) + ch * 16);
  }
  const unsigned ldsw = lds_base + w * 8192;
  auto pieceKV = [&](const char* gb, bool isK, unsigned ldst, int n) {
    const unsigned vo = isK ? voffK[n] : voffV[n];
    const char* g2 = gb + (n >> 2) * 4096;
    const unsigned l2 = ldsw + ldst + (n >> 2) * 4096;
    switch (n & 3) {
      case 0: dma<0, true>(g2, vo, l2); break;
      case 1: dma<1024, true>(g2, vo, l2); break;
      case 2: dma<2048, true>(g2, vo, l2); break;
      default: dma<3072, true>(g2, vo, l2); break;
    }
  };
  auto pieceQ = [&](const char* wgc, const char* xgc, unsigned ldst, int n) {
    if (n < 8) dma<0, false>(wgc, voffW[n], ldsw + ldst + n * 1024);
    else dma<0, false>(xgc, voffX[n - 8], lds_base + ldst + SLOT + (5 * w + n - 8) * 1024);
  };
  STAMP(0);
#pragma unroll
  for (int n = 0; n < 13; ++n) pieceQ(wg, xg, 0, n);
#pragma unroll
  for (int n = 0; n < 13; ++n) pieceQ(wg + 64 * NH * DH * 2, xg + 128, STG, n);
#pragma unroll
  for (int i = 0; i < 192; ++i) AZERO(i);

  {
    const int spw = swap2(pp);
    const unsigned laneW = (unsigned)((8 * hi + q4) * 512 + ((((q4 << 2) | ((blk ^ hi) << 1)) ^ (spw >> 1)) << 4) + 8 * (spw & 1));
    const unsigned lane9 = (unsigned)((8 * g + q4) * 512 + (((8 * w + (pp >> 1)) ^ ((q4 << 2) | ((g & 1) << 1))) << 4) + 8 * (pp & 1));
    const unsigned laneX = (unsigned)(SLOT + (32 * w + r32) * 128 + ((hi ^ ((r32 >> 1) & 7)) << 4));
    const unsigned laneX9 = (unsigned)(SLOT + (128 + i16) * 128 + ((g ^ ((i16 >> 1) & 7)) << 4));
    unsigned stg = 0;
#pragma unroll 1
    for (int c = 0; c < 7; ++c) {
      STAMP(1 + 2 * c);
      if (c < 6) WAITV(13); else WAITV(8);
      BARRIER();
      STAMP(2 + 2 * c);
      bf16x8 fr[12], xq[4], x9q[2];
      unsigned bW0 = lds_base + stg + laneW; OPAQUE(bW0);
      unsigned bX = lds_base + stg + laneX; OPAQUE(bX);
      unsigned bX9 = lds_base + stg + laneX9; OPAQUE(bX9);
      const unsigned bW1 = (bW0 ^ 32) + 2048;
      unsigned b9 = lds_base + stg + lane9; OPAQUE(b9);
      auto ldf = [&](int k) -> bf16x8 {
        if (k < 32) {
          const int ks = k >> 3, d = k & 7;
          return trA2((bW0 ^ ((d & 3) << 6)) + (d >> 2) * 256 + ks * 8192, (bW1 ^ ((d & 3) << 6)) + (d >> 2) * 256 + ks * 8192);
        } else {
          const int ks = (k - 32) >> 2, d = (k - 32) & 3;
          return trA2((b9 ^ (d << 5)) + ks * 16384, ((b9 ^ 32) ^ (d << 5)) + 2048 + ks * 16384);
        }
      };
#pragma unroll
      for (int ks = 0; ks < 4; ++ks) xq[ks] = ldA(bX ^ (ks << 5));
#pragma unroll
      for (int ks = 0; ks < 2; ++ks) x9q[ks] = ldA(bX9 ^ (ks << 6));
#pragma unroll
      for (int k = 0; k < 8; ++k) { const int kk = (k & ~3) + (3 - (k & 3)); fr[kk] = ldf(kk); }
      unsigned nx = stg + 2 * STG; nx = nx >= 3 * STG ? nx - 3 * STG : nx;
      const char* wgc = wg + (size_t)(c + 2) * (64 * NH * DH * 2);
      const char* xgc = xg + (c + 2) * 128;
      auto body = [&](auto mode_c) {
        constexpr int MODE = decltype(mode_c)::value;
        SB();
#pragma unroll
        for (int k = 0; k < 40; ++k) {
          if (k < 32) M32(16 * (k & 7), fr[k % 12], xq[k >> 3]);
          else M16x32(128 + 4 * ((k - 32) & 3), fr[k % 12], x9q[(k - 32) >> 2]);
          { const int kk = (k & ~3) + 8 + (3 - (k & 3)); if (kk < 40) fr[kk % 12] = ldf(kk); }
          if (MODE == 0) { if ((k % 3) == 0 && k / 3 < 13) pieceQ(wgc, xgc, nx, k / 3); }
          else if (MODE == 1) { if ((k & 3) == 1 && k < 32) pieceKV(kg, true, 2 * SLOT, k >> 2); }
          else { if ((k & 1) == 1 && k < 16) pieceKV(kg + SLOT, true, 3 * SLOT, k >> 1);
                 if ((k & 1) == 1 && k >= 16 && k < 32) pieceKV(vg, false, 4 * SLOT, (k - 16) >> 1); }
          SB();
        }
      };
      if (c <= 4) body(std::integral_constant<int, 0>{});
      else if (c == 5) body(std::integral_constant<int, 1>{});
      else body(std::integral_constant<int, 2>{});
      stg += STG; stg = stg >= 3 * STG ? 0 : stg;
    }
  }
  NOPS();
  bf16x8 Qf[16];
#pragma unroll
  for (int s = 0; s < 16; ++s) {
    const int A = 16 * (s >> 1) + 8 * (s & 1);
    Qf[s] = pack8(AREAD(A), AREAD(A + 1), AREAD(A + 2), AREAD(A + 3), AREAD(A + 4), AREAD(A + 5), AREAD(A + 6), AREAD(A + 7));
  }
  RELANE();
  BARRIER();
#pragma unroll
  for (int n = 0; n < 8; ++n) pieceKV(kg + 2 * SLOT, true, 0, n);
#pragma unroll
  for (int d = 0; d < 4; ++d) {
    u32x2 v = {pack2(AREAD(128 + 4 * d), AREAD(129 + 4 * d)), pack2(AREAD(130 + 4 * d), AREAD(131 + 4 * d))};
    const int dh = 64 * w + 16 * d + 4 * g;
    *reinterpret_cast<u32x2*>(smem + SLOT + i16 * 512 + (((dh >> 3) ^ i16) << 4) + ((dh & 4) << 1)) = v;
  }
#pragma unroll
  for (int i = 0; i < 192; ++i) AZERO(i);
  bf16x8 Q9f[8];
  BARRIER();
#pragma unroll
  for (int s = 0; s < 8; ++s) Q9f[s] = ldA(lds_base + SLOT + i16 * 512 + (((4 * s + g) ^ i16) << 4));

  constexpr float CS = 0.0625f * 1.4426950408889634f;
  constexpr float THR = 8.0f / CS;
  float m = -3.0e38f, l = 0.f, m9 = -3.0e38f, l9 = 0.f;
  const unsigned laneK = (unsigned)(r32 * 512 + ((hi ^ (r32 & 15)) << 4));
  const unsigned laneK9 = (unsigned)((16 * w + i16) * 512 + ((g ^ i16) << 4));
  const int spv = swap2(pp);
  const unsigned laneV = (unsigned)((4 * hi + q4) * 512 + ((((q4 << 2) | ((blk ^ hi) << 1)) ^ (spv >> 1)) << 4) + 8 * (spv & 1));
  const unsigned laneV9 = (unsigned)((16 * w + 4 * g + q4) * 512 + (((pp >> 1) ^ ((q4 << 2) | (((g ^ (g >> 1)) & 1) << 1))) << 4) + 8 * (pp & 1));
  int sl = 2;
  float sv[36];
  float mt, mt9;
  bf16x8 pf[4];
  bf16x4 p9f;

  auto s_phase = [&](auto&& extra) {
    bf16x8 fr[12];
    unsigned bK = lds_base + sl * SLOT + laneK; OPAQUE(bK);
    unsigned bK9 = lds_base + sl * SLOT + laneK9; OPAQUE(bK9);
    auto ldf = [&](int k) -> bf16x8 {
      if (k < 32) return ldA((bK ^ ((k & 7) << 5)) + ((k >> 3) & 1) * 256 + (k >> 4) * 16384);
      else return ldA((bK9 ^ ((k & 3) << 6)) + ((k >> 2) & 1) * 256);
    };
#pragma unroll
    for (int k = 0; k < 8; ++k) { const int kk = (k & ~3) + (3 - (k & 3)); fr[kk] = ldf(kk); }
    SB();
#pragma unroll
    for (int k = 0; k < 40; ++k) {
      if (k < 32) { if ((k & 15) == 0) M32Z(192 + 16 * (k >> 4), fr[k % 12], Qf[k & 15]); else M32(192 + 16 * (k >> 4), fr[k % 12], Qf[k & 15]); }
      else { if (k == 32) M16x32Z(224, fr[k % 12], Q9f[k - 32]); else M16x32(224, fr[k % 12], Q9f[k - 32]); }
      { const int kk = (k & ~3) + 8 + (3 - (k & 3)); if (kk < 40) fr[kk % 12] = ldf(kk); }
      extra(k);
      SB();
    }
    sl = sl == 4 ? 0 : sl + 1;
  };
  auto read_scores = [&](int k) {
    if (k < 9) {
#pragma unroll
      for (int i = 0; i < 4; ++i) sv[4 * k + i] = AREAD(192 + 4 * k + i);
    }
    if (k >= 1 && k < 9) {
      const int j = 4 * (k - 1);
      const float v = fmaxf(fmaxf(sv[j], sv[j + 1]), fmaxf(sv[j + 2], sv[j + 3]));
      mt = k == 1 ? v : fmaxf(mt, v);
    }
    if (k == 9) {
      mt9 = fmaxf(fmaxf(sv[32], sv[33]), fmaxf(sv[34], sv[35]));
      mt = fmaxf(mt, __shfl_xor(mt, 32));
      mt9 = fmaxf(mt9, __shfl_xor(mt9, 16));
    }
    if (k == 14) mt9 = fmaxf(mt9, __shfl_xor(mt9, 32));
  };

  WAITV(24);
  BARRIER();
  s_phase([&](int k) { if ((k & 3) == 1 && (k >> 2) < 8) pieceKV(vg + SLOT, false, 1 * SLOT, k >> 2); });
  NOPS();
#pragma unroll
  for (int k = 0; k < 15; ++k) read_scores(k);

#pragma unroll 1
  for (int t = 0; t < 8; ++t) {
    STAMP(15 + 2 * (t));
    WAITV(24);
    BARRIER();
    STAMP(16 + 2 * (t));
    if (__any((mt - m > THR) || (mt9 - m9 > THR))) {
      const float mn = fmaxf(m, mt), mn9 = fmaxf(m9, mt9);
      const float al = __builtin_amdgcn_exp2f((m - mn) * CS), al9 = __builtin_amdgcn_exp2f((m9 - mn9) * CS);
      m = mn; m9 = mn9; l *= al; l9 *= al9;
#pragma unroll
      for (int i = 0; i < 128; ++i) ASCALE(i, al);
#pragma unroll
      for (int i = 128; i < 192; ++i) ASCALE(i, al9);
    }
    float mc = m * CS, mc9 = m9 * CS;
    unsigned pk[18];
    auto sm = [&](int pr) {
      const float c_ = pr < 16 ? mc : mc9;
      const float e0 = __builtin_amdgcn_exp2f(fmaf(sv[2 * pr], CS, -c_)), e1 = __builtin_amdgcn_exp2f(fmaf(sv[2 * pr + 1], CS, -c_));
      if (pr < 16) l += e0 + e1; else l9 += e0 + e1;
      pk[pr] = pack2(e0, e1);
    };
    {
      if (t == 5) {
#pragma unroll
        for (int n = 0; n < 8; ++n) voffK[n] = 0;
      }
      const int ps = sl == 0 ? 4 : sl - 1;
      const char* gk = kg + (size_t)(t < 5 ? t + 3 : 0) * SLOT;
      s_phase([&](int k) {
        if ((k & 1) == 0 && (k >> 1) < 18) sm(k >> 1);
        if ((k & 3) == 1 && (k >> 2) < 8) pieceKV(gk, true, ps * SLOT, k >> 2);
      });
    }
#pragma unroll
    for (int i = 0; i < 4; ++i) {
      u32x4 wv = {pk[4 * i], pk[4 * i + 1], pk[4 * i + 2], pk[4 * i + 3]};
      pf[i] = __builtin_bit_cast(bf16x8, wv);
    }
    {
      u32x2 wv = {pk[16], pk[17]};
      p9f = __builtin_bit_cast(bf16x4, wv);
    }
    STAMP(31 + 2 * (t));
    WAITV(24);
    BARRIER();
    STAMP(32 + 2 * (t));
    {
      bf16x8 fr[12];
      unsigned bV0 = lds_base + sl * SLOT + laneV; OPAQUE(bV0);
      const unsigned bV1 = (bV0 ^ 32) + 4096;
      unsigned bV9 = lds_base + sl * SLOT + laneV9; OPAQUE(bV9);
      auto ldf = [&](int k) -> bf16x8 {
        if (k < 32) {
          const int kp = k >> 3, d = k & 7;
          return trA2((bV0 ^ ((d & 3) << 6)) + (d >> 2) * 256 + kp * 8192, (bV1 ^ ((d & 3) << 6)) + (d >> 2) * 256 + kp * 8192);
        } else {
          const int d = k - 32;
          return trA2((bV9 ^ ((d & 3) << 6)) + (d >> 2) * 256, ((bV9 ^ 32) ^ ((d & 3) << 6)) + (d >> 2) * 256);
        }
      };
#pragma unroll
      for (int k = 0; k < 8; ++k) { const int kk = (k & ~3) + (3 - (k & 3)); fr[kk] = ldf(kk); }
      if (t == 6) {
#pragma unroll
        for (int n = 0; n < 8; ++n) voffV[n] = 0;
      }
      const int ps = sl == 0 ? 4 : sl - 1;
      const char* gv = vg + (size_t)(t < 6 ? t + 2 : 0) * SLOT;
      SB();
#pragma unroll
      for (int k = 0; k < 40; ++k) {
        if (k < 32) M32(16 * (k & 7), fr[k % 12], pf[k >> 3]);
        else {
          const int d = k - 32;
          bf16x4 lo = __builtin_shufflevector(fr[k % 12], fr[k % 12], 0, 1, 2, 3), hi4 = __builtin_shufflevector(fr[k % 12], fr[k % 12], 4, 5, 6, 7);
          M16x16(128 + 8 * d, lo, p9f);
          M16x16(132 + 8 * d, hi4, p9f);
        }
        { const int kk = (k & ~3) + 8 + (3 - (k & 3)); if (kk < 40) fr[kk % 12] = ldf(kk); }
        if ((k & 3) == 1 && (k >> 2) < 8) pieceKV(gv, false, ps * SLOT, k >> 2);
        if (k >= 2 && k < 17) read_scores(k - 2);
        SB();
      }
      sl = sl == 4 ? 0 : sl + 1;
    }
  }
  WAITV(0);
  NOPS();
  l += __shfl_xor(l, 32);
  l9 += __shfl_xor(l9, 16);
  l9 += __shfl_xor(l9, 32);
  const float inv_l = 1.0f / l;

  STAMP(47);
  char* ofp = (char*)Of + (size_t)p * OF_PER_P * 2;
  {
    char* dst = ofp + ((size_t)(4 * b + w) * 256 + h * 16) * 1024 + lane * 16;
#pragma unroll
    for (int s = 0; s < 16; ++s) {
      const int A = 16 * (s >> 1) + 8 * (s & 1);
      bf16x8 v = pack8(AREAD(A) * inv_l, AREAD(A + 1) * inv_l, AREAD(A + 2) * inv_l, AREAD(A + 3) * inv_l,
                       AREAD(A + 4) * inv_l, AREAD(A + 5) * inv_l, AREAD(A + 6) * inv_l, AREAD(A + 7) * inv_l);
      *reinterpret_cast<bf16x8*>(dst + s * 1024) = v;
    }
  }
  RELANE();
  BARRIER();
  {
    float* st = reinterpret_cast<float*>(smem + 4 * 16 * 1040);
#pragma unroll
    for (int d = 0; d < 16; ++d) {
      f32x4 v = {AREAD(128 + 4 * d), AREAD(129 + 4 * d), AREAD(130 + 4 * d), AREAD(131 + 4 * d)};
      *reinterpret_cast<f32x4*>(smem + (w * 16 + i16) * 1040 + (16 * d + 4 * g) * 4) = v;
    }
    if (g == 0) { st[(w * 16 + i16) * 2] = m9 * CS; st[(w * 16 + i16) * 2 + 1] = l9; }
    BARRIER();
#pragma unroll
    for (int it = 0; it < 2; ++it) {
      const int item = tid + 256 * it;
      const int q = item >> 5, ck = item & 31;
      float mw[4], lw[4];
#pragma unroll
      for (int ww = 0; ww < 4; ++ww) { mw[ww] = st[(ww * 16 + q) * 2]; lw[ww] = st[(ww * 16 + q) * 2 + 1]; }
      const float M = fmaxf(fmaxf(mw[0], mw[1]), fmaxf(mw[2], mw[3]));
      float f[4], L = 0.f;
#pragma unroll
      for (int ww = 0; ww < 4; ++ww) { f[ww] = __builtin_amdgcn_exp2f(mw[ww] - M); L += f[ww] * lw[ww]; }
      const float il = 1.0f / L;
      float o[8];
#pragma unroll
      for (int j = 0; j < 8; ++j) o[j] = 0.f;
#pragma unroll
      for (int ww = 0; ww < 4; ++ww) {
        const f32x4 a = *reinterpret_cast<const f32x4*>(smem + (ww * 16 + q) * 1040 + ck * 32);
        const f32x4 c2 = *reinterpret_cast<const f32x4*>(smem + (ww * 16 + q) * 1040 + ck * 32 + 16);
#pragma unroll
        for (int j = 0; j < 4; ++j) { o[j] += f[ww] * a[j]; o[4 + j] += f[ww] * c2[j]; }
      }
      bf16x8 v = pack8(o[0] * il, o[1] * il, o[2] * il, o[3] * il, o[4] * il, o[5] * il, o[6] * il, o[7] * il);
      char* dst = ofp + ((size_t)(32 + (b >> 1)) * 256 + h * 16 + (ck >> 1)) * 1024 + (16 * (b & 1) + q + 32 * (ck & 1)) * 16;
      *reinterpret_cast<bf16x8*>(dst) = v;
    }
  }
  STAMP(48);
}

constexpr int K2_NBUF = 5;
constexpr int K2_WAVE_LDS = K2_NBUF * 4096;
DI bf16x8 gload16_asm(const char* p) {
  bf16x8 r;
  asm volatile("global_load_dwordx4 %0, %1, off" : "=v"(r) : "v"(p) : "memory");
  return r;
}
__global__ __launch_bounds__(512, 2) void oproj_kernel(const __hip_bfloat16* __restrict__ Of, const __hip_bfloat16* __restrict__ woA,
                                                       const __hip_bfloat16* __restrict__ woB, __hip_bfloat16* __restrict__ out) {
  extern __shared__ __attribute__((aligned(16))) char smem[];
  const int tid = threadIdx.x, lane = tid & 63;
  const int w = __builtin_amdgcn_readfirstlane(tid >> 6);
  const int T = (blockIdx.x & 7) * 32 + (blockIdx.x >> 3);
  if (T >= 252) return;
  const int p = T / 126, rem = T % 126, mg = rem / 7, ng = rem % 7;
  const char* of = (const char*)Of + (size_t)p * OF_PER_P * 2 + ((size_t)(2 * mg) * 256 + w * 32) * 1024 + lane * 16;
  const char* wo = (const char*)(p ? woB : woA) + (size_t)(w * 512) * (DM * 2) + ng * 128;
  const int hi = lane >> 5, blk = (lane >> 4) & 1, q4 = (lane & 15) >> 2, pp = lane & 3;
  const unsigned lds_base = (unsigned)(size_t)(lds_b4_ptr)smem + w * K2_WAVE_LDS;
  const unsigned voff = (unsigned)((lane >> 3) * (DM * 2) + (((lane & 7) ^ (((lane >> 4) & 1) << 2)) << 4));

  bf16x8 ob[K2_NBUF][2][2];
  auto issue = [&](int ck) {
#pragma unroll
    for (int mt = 0; mt < 2; ++mt)
#pragma unroll
      for (int ks = 0; ks < 2; ++ks) ob[ck % K2_NBUF][mt][ks] = gload16_asm(of + ((size_t)mt * 256 + 2 * ck + ks) * 1024);
#pragma unroll
    for (int n = 0; n < 4; ++n)
      glds16(wo + (size_t)(32 * ck + 8 * n) * (DM * 2), voff, lds_base + (ck % K2_NBUF) * 4096 + n * 1024);
  };
  f32x16 acc[2][2];
#pragma unroll
  for (int a = 0; a < 2; ++a)
#pragma unroll
    for (int c = 0; c < 2; ++c) acc[a][c] = f32x16{};
  issue(0); issue(1); issue(2); issue(3);
#pragma unroll
  for (int ck = 0; ck < 16; ++ck) {
    if (ck + 4 < 16) { issue(ck + 4); WAITV(32); }
    else if (ck == 12) WAITV(24); else if (ck == 13) WAITV(16); else if (ck == 14) WAITV(8); else WAITV(0);
    {
      bf16x8 (&o)[2][2] = ob[ck % K2_NBUF];
      asm volatile("" : "+v"(o[0][0]), "+v"(o[0][1]), "+v"(o[1][0]), "+v"(o[1][1]));
    }
    const unsigned sb = w * K2_WAVE_LDS + (ck % K2_NBUF) * 4096;
    bf16x8 fa[2][2];
#pragma unroll
    for (int ks = 0; ks < 2; ++ks)
#pragma unroll
      for (int nt = 0; nt < 2; ++nt) {
        const int row0 = 16 * ks + 8 * hi + q4, row1 = row0 + 4;
        const int ch = 4 * nt + 2 * blk + (pp >> 1);
        fa[ks][nt] = tr_read2(smem, sb + row0 * 128 + ((ch ^ (((row0 >> 1) & 1) << 2)) << 4) + 8 * (pp & 1),
                              sb + row1 * 128 + ((ch ^ (((row1 >> 1) & 1) << 2)) << 4) + 8 * (pp & 1));
      }
#pragma unroll
    for (int ks = 0; ks < 2; ++ks)
#pragma unroll
      for (int nt = 0; nt < 2; ++nt)
#pragma unroll
        for (int mt = 0; mt < 2; ++mt) acc[nt][mt] = mfma32(fa[ks][nt], ob[ck % K2_NBUF][mt][ks], acc[nt][mt]);
    asm volatile("s_waitcnt lgkmcnt(0)" ::: "memory");
  }
  __syncthreads();
  const int r32 = lane & 31;
#pragma unroll
  for (int nt = 0; nt < 2; ++nt)
#pragma unroll
    for (int mt = 0; mt < 2; ++mt)
#pragma unroll
      for (int qd = 0; qd < 4; ++qd) {
        f32x4 v = {acc[nt][mt][4 * qd], acc[nt][mt][4 * qd + 1], acc[nt][mt][4 * qd + 2], acc[nt][mt][4 * qd + 3]};
        *reinterpret_cast<f32x4*>(smem + ((size_t)w * 64 + 32 * mt + r32) * 272 + (32 * nt + 8 * qd + 4 * hi) * 4) = v;
      }
  __syncthreads();
  {
    const int m = tid >> 3, nc = tid & 7;
    float o[8];
#pragma unroll
    for (int j = 0; j < 8; ++j) o[j] = 0.f;
#pragma unroll
    for (int ww = 0; ww < 8; ++ww) {
      const f32x4 a = *reinterpret_cast<const f32x4*>(smem + ((size_t)ww * 64 + m) * 272 + nc * 32);
      const f32x4 c = *reinterpret_cast<const f32x4*>(smem + ((size_t)ww * 64 + m) * 272 + nc * 32 + 16);
#pragma unroll
      for (int j = 0; j < 4; ++j) { o[j] += a[j]; o[4 + j] += c[j]; }
    }
    const int mt = 2 * mg + (m >> 5), r = m & 31;
    int row;
    if (mt < 32) row = (mt >> 2) * SQ + (mt & 3) * 32 + r;
    else row = (2 * (mt - 32) + (r >> 4)) * SQ + 128 + (r & 15);
    bf16x8 v = pack8(o[0], o[1], o[2], o[3], o[4], o[5], o[6], o[7]);
    *reinterpret_cast<bf16x8*>((char*)out + ((size_t)row * (2 * DM) + p * DM + ng * 64 + nc * 8) * 2) = v;
  }
}

extern "C" void kernel_launch(void* const* d_in, const int* in_sizes, int n_in, void* d_out, int out_size, void* d_ws,
                              size_t ws_size, hipStream_t stream) {
  const __hip_bfloat16* xA = (const __hip_bfloat16*)d_in[0];
  const __hip_bfloat16* kA = (const __hip_bfloat16*)d_in[1];
  const __hip_bfloat16* vA = (const __hip_bfloat16*)d_in[2];
  const __hip_bfloat16* wqA = (const __hip_bfloat16*)d_in[3];
  const __hip_bfloat16* woA = (const __hip_bfloat16*)d_in[4];
  const __hip_bfloat16* xB = (const __hip_bfloat16*)d_in[5];
  const __hip_bfloat16* kB = (const __hip_bfloat16*)d_in[6];
  const __hip_bfloat16* vB = (const __hip_bfloat16*)d_in[7];
  const __hip_bfloat16* wqB = (const __hip_bfloat16*)d_in[8];
  const __hip_bfloat16* woB = (const __hip_bfloat16*)d_in[9];
  __hip_bfloat16* Of = (__hip_bfloat16*)d_ws;
  hipFuncSetAttribute((const void*)attn_kernel, hipFuncAttributeMaxDynamicSharedMemorySize, LDS_BYTES);
  hipFuncSetAttribute((const void*)oproj_kernel, hipFuncAttributeMaxDynamicSharedMemorySize, 8 * K2_WAVE_LDS);
  attn_kernel<<<256, 256, LDS_BYTES, stream>>>(xA, kA, vA, wqA, xB, kB, vB, wqB, Of
  );
  oproj_kernel<<<256, 512, 8 * K2_WAVE_LDS, stream>>>(Of, woA, woB, (__hip_bfloat16*)d_out);
}
```

```cpp
#include <hip/hip_runtime.h>
#include <hip/hip_bf16.h>
#include <stdint.h>
#include <type_traits>
#define ILQ 2
#define ILF 4

typedef short bf16x8 __attribute__((ext_vector_type(8)));
typedef short bf16x4 __attribute__((ext_vector_type(4)));
typedef float f32x16 __attribute__((ext_vector_type(16)));
typedef float f32x4 __attribute__((ext_vector_type(4)));
typedef unsigned u32x4 __attribute__((ext_vector_type(4)));
typedef unsigned u32x2 __attribute__((ext_vector_type(2)));
typedef __bf16 bf2_t __attribute__((ext_vector_type(2)));
typedef __attribute__((address_space(3))) bf16x4* lds_b4_ptr;

#define DI __device__ __forceinline__

constexpr int NB = 8, SQ = 144, DM = 448, NH = 16, SK = 512, DH = 256;
constexpr int SLOT = 32768;
constexpr int LDS_BYTES = 5 * SLOT;
constexpr size_t OF_PER_P = (size_t)36 * 256 * 512;

DI unsigned pack2(float a, float b) {
  bf2_t v = {(__bf16)a, (__bf16)b};
  return __builtin_bit_cast(unsigned, v);
}
DI bf16x8 pack8(float a0, float a1, float a2, float a3, float a4, float a5, float a6, float a7) {
  u32x4 w = {pack2(a0, a1), pack2(a2, a3), pack2(a4, a5), pack2(a6, a7)};
  return __builtin_bit_cast(bf16x8, w);
}
DI f32x16 mfma32(bf16x8 a, bf16x8 b, f32x16 c) { return __builtin_amdgcn_mfma_f32_32x32x16_bf16(a, b, c, 0, 0, 0); }
DI f32x4 mfma16x32(bf16x8 a, bf16x8 b, f32x4 c) { return __builtin_amdgcn_mfma_f32_16x16x32_bf16(a, b, c, 0, 0, 0); }
DI f32x4 mfma16x16(bf16x4 a, bf16x4 b, f32x4 c) { return __builtin_amdgcn_mfma_f32_16x16x16bf16_1k(a, b, c, 0, 0, 0); }

#define WAITV(n) asm volatile("s_waitcnt vmcnt(%0)" ::"n"(n) : "memory")
#define SB() __builtin_amdgcn_sched_barrier(0)
#define STAMP(i)
#define BARRIER() do { asm volatile("s_waitcnt lgkmcnt(0)" ::: "memory"); __builtin_amdgcn_s_barrier(); asm volatile("" ::: "memory"); } while (0)

DI bf16x4 tr_read(const char* smem, unsigned off) {
  return __builtin_amdgcn_ds_read_tr16_b64_v4i16((lds_b4_ptr)(smem + off));
}
DI bf16x8 tr_read2(const char* smem, unsigned off0, unsigned off1) {
  bf16x4 lo = tr_read(smem, off0), hi = tr_read(smem, off1);
  return __builtin_shufflevector(lo, hi, 0, 1, 2, 3, 4, 5, 6, 7);
}
DI void glds16(const char* gbase, unsigned voff, unsigned ldst) {
  unsigned keep;
  asm volatile("s_mov_b32 %0, m0\n\ts_mov_b32 m0, %3\n\ts_nop 0\n\tglobal_load_lds_dwordx4 %1, %2\n\ts_mov_b32 m0, %0"
               : "=&s"(keep) : "v"(voff), "s"(gbase), "s"(ldst) : "memory");
}
DI void glds16nt(const char* gbase, unsigned voff, unsigned ldst) {
  unsigned keep;
  asm volatile("s_mov_b32 %0, m0\n\ts_mov_b32 m0, %3\n\ts_nop 0\n\tglobal_load_lds_dwordx4 %1, %2 nt\n\ts_mov_b32 m0, %0"
               : "=&s"(keep) : "v"(voff), "s"(gbase), "s"(ldst) : "memory");
}
DI bf16x8 lds128(const char* smem, unsigned off) { return *reinterpret_cast<const bf16x8*>(smem + off); }

typedef __attribute__((address_space(3))) bf16x8* lds_b8_ptr;
DI bf16x4 trA(unsigned addr) { return __builtin_amdgcn_ds_read_tr16_b64_v4i16(reinterpret_cast<lds_b4_ptr>(addr)); }
DI bf16x8 trA2(unsigned a0, unsigned a1) {
  bf16x4 lo = trA(a0), hi = trA(a1);
  return __builtin_shufflevector(lo, hi, 0, 1, 2, 3, 4, 5, 6, 7);
}
DI bf16x8 ldA(unsigned addr) { return *reinterpret_cast<lds_b8_ptr>(addr); }
DI void ascale(float& a, float s) {
  float tmp;
  asm volatile("v_accvgpr_read_b32 %1, %0\n\tv_mul_f32 %1, %1, %2\n\tv_accvgpr_write_b32 %0, %1" : "+a"(a), "=&v"(tmp) : "v"(s));
}
DI void amfma32(f32x16& c, bf16x8 a, bf16x8 b) { asm volatile("v_mfma_f32_32x32x16_bf16 %0, %1, %2, %0" : "+a"(c) : "v"(a), "v"(b)); }
DI void amfma32z(f32x16& c, bf16x8 a, bf16x8 b) { asm volatile("v_mfma_f32_32x32x16_bf16 %0, %1, %2, 0" : "=a"(c) : "v"(a), "v"(b)); }
DI void amfma16x32(f32x4& c, bf16x8 a, bf16x8 b) { asm volatile("v_mfma_f32_16x16x32_bf16 %0, %1, %2, %0" : "+a"(c) : "v"(a), "v"(b)); }
DI void amfma16x32z(f32x4& c, bf16x8 a, bf16x8 b) { asm volatile("v_mfma_f32_16x16x32_bf16 %0, %1, %2, 0" : "=a"(c) : "v"(a), "v"(b)); }
DI void amfma16x16(f32x4& c, bf16x4 a, bf16x4 b) { asm volatile("v_mfma_f32_16x16x16_bf16 %0, %1, %2, %0" : "+a"(c) : "v"(a), "v"(b)); }
#define MFMA_DRAIN() asm volatile("s_nop 15\n\ts_nop 15" ::: "memory")
#define OPAQUE(x) asm volatile("" : "+v"(x))

DI int swzT(int row) { return ((row & 3) << 2) | ((((row >> 2) ^ (row >> 3)) & 1) << 1); }
DI int swap2(int p) { return ((p & 1) << 1) | (p >> 1); }

constexpr int STG = 53248;

#define ALL_AGPRS "a0", "a1", "a2", "a3", "a4", "a5", "a6", "a7", "a8", "a9", "a10", "a11", "a12", "a13", "a14", "a15", "a16", "a17", "a18", "a19", "a20", "a21", "a22", "a23", "a24", "a25", "a26", "a27", "a28", "a29", "a30", "a31", "a32", "a33", "a34", "a35", "a36", "a37", "a38", "a39", "a40", "a41", "a42", "a43", "a44", "a45", "a46", "a47", "a48", "a49", "a50", "a51", "a52", "a53", "a54", "a55", "a56", "a57", "a58", "a59", "a60", "a61", "a62", "a63", "a64", "a65", "a66", "a67", "a68", "a69", "a70", "a71", "a72", "a73", "a74", "a75", "a76", "a77", "a78", "a79", "a80", "a81", "a82", "a83", "a84", "a85", "a86", "a87", "a88", "a89", "a90", "a91", "a92", "a93", "a94", "a95", "a96", "a97", "a98", "a99", "a100", "a101", "a102", "a103", "a104", "a105", "a106", "a107", "a108", "a109", "a110", "a111", "a112", "a113", "a114", "a115", "a116", "a117", "a118", "a119", "a120", "a121", "a122", "a123", "a124", "a125", "a126", "a127", "a128", "a129", "a130", "a131", "a132", "a133", "a134", "a135", "a136", "a137", "a138", "a139", "a140", "a141", "a142", "a143", "a144", "a145", "a146", "a147", "a148", "a149", "a150", "a151", "a152", "a153", "a154", "a155", "a156", "a157", "a158", "a159", "a160", "a161", "a162", "a163", "a164", "a165", "a166", "a167", "a168", "a169", "a170", "a171", "a172", "a173", "a174", "a175", "a176", "a177", "a178", "a179", "a180", "a181", "a182", "a183", "a184", "a185", "a186", "a187", "a188", "a189", "a190", "a191", "a192", "a193", "a194", "a195", "a196", "a197", "a198", "a199", "a200", "a201", "a202", "a203", "a204", "a205", "a206", "a207", "a208", "a209", "a210", "a211", "a212", "a213", "a214", "a215", "a216", "a217", "a218", "a219", "a220", "a221", "a222", "a223", "a224", "a225", "a226", "a227", "a228", "a229", "a230", "a231", "a232", "a233", "a234", "a235", "a236", "a237", "a238", "a239", "a240", "a241", "a242", "a243", "a244", "a245", "a246", "a247", "a248", "a249", "a250", "a251", "a252", "a253", "a254", "a255"
DI void M32(int A, bf16x8 a, bf16x8 b) { asm volatile("v_mfma_f32_32x32x16_bf16 a[%c2:%c3], %0, %1, a[%c2:%c3]" ::"v"(a), "v"(b), "i"(A), "i"(A + 15) : ALL_AGPRS); }
DI void M32Z(int A, bf16x8 a, bf16x8 b) { asm volatile("v_mfma_f32_32x32x16_bf16 a[%c2:%c3], %0, %1, 0" ::"v"(a), "v"(b), "i"(A), "i"(A + 15) : ALL_AGPRS); }
DI void M16x32(int A, bf16x8 a, bf16x8 b) { asm volatile("v_mfma_f32_16x16x32_bf16 a[%c2:%c3], %0, %1, a[%c2:%c3]" ::"v"(a), "v"(b), "i"(A), "i"(A + 3) : ALL_AGPRS); }
DI void M16x32Z(int A, bf16x8 a, bf16x8 b) { asm volatile("v_mfma_f32_16x16x32_bf16 a[%c2:%c3], %0, %1, 0" ::"v"(a), "v"(b), "i"(A), "i"(A + 3) : ALL_AGPRS); }
DI void M16x16(int A, bf16x4 a, bf16x4 b) { asm volatile("v_mfma_f32_16x16x16_bf16 a[%c2:%c3], %0, %1, a[%c2:%c3]" ::"v"(a), "v"(b), "i"(A), "i"(A + 3) : ALL_AGPRS); }
DI float AREAD(int A) { float r; asm volatile("v_accvgpr_read_b32 %0, a%c1" : "=v"(r) : "i"(A)); return r; }
DI void AZERO(int A) { asm volatile("v_accvgpr_write_b32 a%c0, 0" ::"i"(A) : ALL_AGPRS); }
DI void ASCALE(int A, float s) {
  float t;
  asm volatile("v_accvgpr_read_b32 %0, a%c2\n\tv_mul_f32 %0, %0, %1\n\tv_accvgpr_write_b32 a%c2, %0" : "=&v"(t) : "v"(s), "i"(A));
}
#define NOPS() asm volatile("s_nop 15\n\ts_nop 15")
template <int OFF, bool NT>
DI void dma(const char* gbase, unsigned voff, unsigned ldst) {
  if (NT) asm volatile("s_mov_b32 m0, %2\n\ts_nop 0\n\tglobal_load_lds_dwordx4 %0, %1 offset:%c3 nt" ::"v"(voff), "s"(gbase), "s"(ldst), "i"(OFF) : "memory");
  else asm volatile("s_mov_b32 m0, %2\n\ts_nop 0\n\tglobal_load_lds_dwordx4 %0, %1 offset:%c3" ::"v"(voff), "s"(gbase), "s"(ldst), "i"(OFF) : "memory");
}

__global__ __launch_bounds__(256, 1) void attn_kernel(
    const __hip_bfloat16* __restrict__ xA, const __hip_bfloat16* __restrict__ kA, const __hip_bfloat16* __restrict__ vA,
    const __hip_bfloat16* __restrict__ wqA, const __hip_bfloat16* __restrict__ xB, const __hip_bfloat16* __restrict__ kB,
    const __hip_bfloat16* __restrict__ vB, const __hip_bfloat16* __restrict__ wqB, __hip_bfloat16* __restrict__ Of,
    const __hip_bfloat16* __restrict__ woA, const __hip_bfloat16* __restrict__ woB
    ) {
  extern __shared__ __attribute__((aligned(16))) char smem[];
  asm volatile("" ::: "a0", "a64", "a128", "a192", "a227");
  const int tid = threadIdx.x, lane = tid & 63;
  const int w = __builtin_amdgcn_readfirstlane(tid >> 6);
  const int bid = blockIdx.x, xcd = bid & 7, idx = bid >> 3;
  const int ph = xcd * 4 + (idx >> 3), p = ph >> 4, h = ph & 15, b = idx & 7;
  const char* xg = (const char*)(p ? xB : xA) + (size_t)b * SQ * DM * 2;
  const char* wg = (const char*)(p ? wqB : wqA) + (size_t)h * DH * 2;
  const char* kg = (const char*)(p ? kB : kA) + (size_t)(b * NH + h) * SK * DH * 2;
  const char* vg = (const char*)(p ? vB : vA) + (size_t)(b * NH + h) * SK * DH * 2;

  int r32 = lane & 31, hi = lane >> 5, i16 = lane & 15, g = lane >> 4;
  int blk = (lane >> 4) & 1, q4 = (lane & 15) >> 2, pp = lane & 3;
#define RELANE() do { int l_ = lane; asm volatile("" : "+v"(l_)); r32 = l_ & 31; hi = l_ >> 5; i16 = l_ & 15; g = l_ >> 4; \
    blk = (l_ >> 4) & 1; q4 = (l_ & 15) >> 2; pp = l_ & 3; } while (0)

  const unsigned lds_base = (unsigned)(size_t)(lds_b4_ptr)smem;
  unsigned voffK[8], voffV[8], voffW[8], voffX[5];
#pragma unroll
  for (int n = 0; n < 8; ++n) {
    const unsigned xk = (unsigned)(2 * n), xv = (unsigned)((((2 * n) & 3) << 2) | ((((n >> 1) ^ (n >> 2)) & 1) << 1));
    voffK[n] = (unsigned)((16 * w + hi) * 512 + (((r32 ^ hi) ^ xk) << 4));
    voffV[n] = (unsigned)((16 * w + hi) * 512 + (((r32 ^ (hi << 2)) ^ xv) << 4));
    voffW[n] = (unsigned)((16 * w + hi + 2 * n) * 8192 + (((r32 ^ (hi << 2)) ^ xv) << 4));
  }
#pragma unroll
  for (int n = 0; n < 5; ++n) {
    const int gi = 5 * w + n;
    int row = 8 * gi + (lane >> 3);
    const int ch = (lane & 7) ^ ((row >> 1) & 7);
    row = row < SQ ? row : SQ - 1;
    voffX[n] = (unsigned)(row * (DM * 2) + ch * 16);
  }
  const unsigned ldsw = lds_base + w * 8192;
  auto pieceKV = [&](const char* gb, bool isK, unsigned ldst, int n) {
    const unsigned vo = isK ? voffK[n] : voffV[n];
    const char* g2 = gb + (n >> 2) * 4096;
    const unsigned l2 = ldsw + ldst + (n >> 2) * 4096;
    switch (n & 3) {
      case 0: dma<0, true>(g2, vo, l2); break;
      case 1: dma<1024, true>(g2, vo, l2); break;
      case 2: dma<2048, true>(g2, vo, l2); break;
      default: dma<3072, true>(g2, vo, l2); break;
    }
  };
  auto pieceQ = [&](const char* wgc, const char* xgc, unsigned ldst, int n) {
    if (n < 8) dma<0, false>(wgc, voffW[n], ldsw + ldst + n * 1024);
    else dma<0, false>(xgc, voffX[n - 8], lds_base + ldst + SLOT + (5 * w + n - 8) * 1024);
  };
  STAMP(0);
  {
    const char* wop = (const char*)(p ? woB : woA) + (size_t)(b * NH + h) * 28672 + w * 7168;
#pragma unroll
    for (int n = 0; n < 7; ++n) dma<0, false>(wop + n * 1024, (unsigned)(lane * 16), lds_base + 3 * STG + w * 1024);
  }
#pragma unroll
  for (int n = 0; n < 13; ++n) pieceQ(wg, xg, 0, n);
#pragma unroll
  for (int n = 0; n < 13; ++n) pieceQ(wg + 64 * NH * DH * 2, xg + 128, STG, n);
#pragma unroll
  for (int i = 0; i < 192; ++i) AZERO(i);

  {
    const int spw = swap2(pp);
    const unsigned laneW = (unsigned)((8 * hi + q4) * 512 + ((((q4 << 2) | ((blk ^ hi) << 1)) ^ (spw >> 1)) << 4) + 8 * (spw & 1));
    const unsigned lane9 = (unsigned)((8 * g + q4) * 512 + (((8 * w + (pp >> 1)) ^ ((q4 << 2) | ((g & 1) << 1))) << 4) + 8 * (pp & 1));
    const unsigned laneX = (unsigned)(SLOT + (32 * w + r32) * 128 + ((hi ^ ((r32 >> 1) & 7)) << 4));
    const unsigned laneX9 = (unsigned)(SLOT + (128 + i16) * 128 + ((g ^ ((i16 >> 1) & 7)) << 4));
    unsigned stg = 0;
#pragma unroll 1
    for (int c = 0; c < 7; ++c) {
      STAMP(1 + 2 * c);
      if (c < 6) WAITV(13); else WAITV(8);
      BARRIER();
      STAMP(2 + 2 * c);
      bf16x8 fr[12], xq[4], x9q[2];
      unsigned bW0 = lds_base + stg + laneW; OPAQUE(bW0);
      unsigned bX = lds_base + stg + laneX; OPAQUE(bX);
      unsigned bX9 = lds_base + stg + laneX9; OPAQUE(bX9);
      const unsigned bW1 = (bW0 ^ 32) + 2048;
      unsigned b9 = lds_base + stg + lane9; OPAQUE(b9);
      auto ldf = [&](int k) -> bf16x8 {
        if (k < 32) {
          const int ks = k >> 3, d = k & 7;
          return trA2((bW0 ^ ((d & 3) << 6)) + (d >> 2) * 256 + ks * 8192, (bW1 ^ ((d & 3) << 6)) + (d >> 2) * 256 + ks * 8192);
        } else {
          const int ks = (k - 32) >> 2, d = (k - 32) & 3;
          return trA2((b9 ^ (d << 5)) + ks * 16384, ((b9 ^ 32) ^ (d << 5)) + 2048 + ks * 16384);
        }
      };
#pragma unroll
      for (int ks = 0; ks < 4; ++ks) xq[ks] = ldA(bX ^ (ks << 5));
#pragma unroll
      for (int ks = 0; ks < 2; ++ks) x9q[ks] = ldA(bX9 ^ (ks << 6));
#pragma unroll
      for (int k = 0; k < 8; ++k) { const int kk = (k & ~3) + (3 - (k & 3)); fr[kk] = ldf(kk); }
      unsigned nx = stg + 2 * STG; nx = nx >= 3 * STG ? nx - 3 * STG : nx;
      const char* wgc = wg + (size_t)(c + 2) * (64 * NH * DH * 2);
      const char* xgc = xg + (c + 2) * 128;
      auto body = [&](auto mode_c) {
        constexpr int MODE = decltype(mode_c)::value;
        SB();
#pragma unroll
        for (int k = 0; k < 40; ++k) {
          if (k < 32) M32(16 * (k & 7), fr[k % 12], xq[k >> 3]);
          else M16x32(128 + 4 * ((k - 32) & 3), fr[k % 12], x9q[(k - 32) >> 2]);
          { const int kk = (k & ~3) + 8 + (3 - (k & 3)); if (kk < 40) fr[kk % 12] = ldf(kk); }
          if (MODE == 0) { if ((k % 3) == 0 && k / 3 < 13) pieceQ(wgc, xgc, nx, k / 3); }
          else if (MODE == 1) { if ((k & 3) == 1 && k < 32) pieceKV(kg, true, 2 * SLOT, k >> 2); }
          else { if ((k & 1) == 1 && k < 16) pieceKV(kg + SLOT, true, 3 * SLOT, k >> 1);
                 if ((k & 1) == 1 && k >= 16 && k < 32) pieceKV(vg, false, 4 * SLOT, (k - 16) >> 1); }
          SB();
        }
      };
      if (c <= 4) body(std::integral_constant<int, 0>{});
      else if (c == 5) body(std::integral_constant<int, 1>{});
      else body(std::integral_constant<int, 2>{});
      stg += STG; stg = stg >= 3 * STG ? 0 : stg;
    }
  }
  NOPS();
  bf16x8 Qf[16];
#pragma unroll
  for (int s = 0; s < 16; ++s) {
    const int A = 16 * (s >> 1) + 8 * (s & 1);
    Qf[s] = pack8(AREAD(A), AREAD(A + 1), AREAD(A + 2), AREAD(A + 3), AREAD(A + 4), AREAD(A + 5), AREAD(A + 6), AREAD(A + 7));
  }
  RELANE();
  BARRIER();
#pragma unroll
  for (int n = 0; n < 8; ++n) pieceKV(kg + 2 * SLOT, true, 0, n);
#pragma unroll
  for (int d = 0; d < 4; ++d) {
    u32x2 v = {pack2(AREAD(128 + 4 * d), AREAD(129 + 4 * d)), pack2(AREAD(130 + 4 * d), AREAD(131 + 4 * d))};
    const int dh = 64 * w + 16 * d + 4 * g;
    *reinterpret_cast<u32x2*>(smem + SLOT + i16 * 512 + (((dh >> 3) ^ i16) << 4) + ((dh & 4) << 1)) = v;
  }
#pragma unroll
  for (int i = 0; i < 192; ++i) AZERO(i);
  bf16x8 Q9f[8];
  BARRIER();
#pragma unroll
  for (int s = 0; s < 8; ++s) Q9f[s] = ldA(lds_base + SLOT + i16 * 512 + (((4 * s + g) ^ i16) << 4));

  constexpr float CS = 0.0625f * 1.4426950408889634f;
  constexpr float THR = 8.0f / CS;
  float m = -3.0e38f, l = 0.f, m9 = -3.0e38f, l9 = 0.f;
  const unsigned laneK = (unsigned)(r32 * 512 + ((hi ^ (r32 & 15)) << 4));
  const unsigned laneK9 = (unsigned)((16 * w + i16) * 512 + ((g ^ i16) << 4));
  const int spv = swap2(pp);
  const unsigned laneV = (unsigned)((4 * hi + q4) * 512 + ((((q4 << 2) | ((blk ^ hi) << 1)) ^ (spv >> 1)) << 4) + 8 * (spv & 1));
  const unsigned laneV9 = (unsigned)((16 * w + 4 * g + q4) * 512 + (((pp >> 1) ^ ((q4 << 2) | (((g ^ (g >> 1)) & 1) << 1))) << 4) + 8 * (pp & 1));
  int sl = 2;
  float sv[36];
  float mt, mt9;
  bf16x8 pf[4];
  bf16x4 p9f;

  auto s_phase = [&](auto&& extra) {
    bf16x8 fr[12];
    unsigned bK = lds_base + sl * SLOT + laneK; OPAQUE(bK);
    unsigned bK9 = lds_base + sl * SLOT + laneK9; OPAQUE(bK9);
    auto ldf = [&](int k) -> bf16x8 {
      if (k < 32) return ldA((bK ^ ((k & 7) << 5)) + ((k >> 3) & 1) * 256 + (k >> 4) * 16384);
      else return ldA((bK9 ^ ((k & 3) << 6)) + ((k >> 2) & 1) * 256);
    };
#pragma unroll
    for (int k = 0; k < 8; ++k) { const int kk = (k & ~3) + (3 - (k & 3)); fr[kk] = ldf(kk); }
    SB();
#pragma unroll
    for (int k = 0; k < 40; ++k) {
      if (k < 32) { if ((k & 15) == 0) M32Z(192 + 16 * (k >> 4), fr[k % 12], Qf[k & 15]); else M32(192 + 16 * (k >> 4), fr[k % 12], Qf[k & 15]); }
      else { if (k == 32) M16x32Z(224, fr[k % 12], Q9f[k - 32]); else M16x32(224, fr[k % 12], Q9f[k - 32]); }
      { const int kk = (k & ~3) + 8 + (3 - (k & 3)); if (kk < 40) fr[kk % 12] = ldf(kk); }
      extra(k);
      SB();
    }
    sl = sl == 4 ? 0 : sl + 1;
  };
  auto read_scores = [&](int k) {
    if (k < 9) {
#pragma unroll
      for (int i = 0; i < 4; ++i) sv[4 * k + i] = AREAD(192 + 4 * k + i);
    }
    if (k >= 1 && k < 9) {
      const int j = 4 * (k - 1);
      const float v = fmaxf(fmaxf(sv[j], sv[j + 1]), fmaxf(sv[j + 2], sv[j + 3]));
      mt = k == 1 ? v : fmaxf(mt, v);
    }
    if (k == 9) {
      mt9 = fmaxf(fmaxf(sv[32], sv[33]), fmaxf(sv[34], sv[35]));
      mt = fmaxf(mt, __shfl_xor(mt, 32));
      mt9 = fmaxf(mt9, __shfl_xor(mt9, 16));
    }
    if (k == 14) mt9 = fmaxf(mt9, __shfl_xor(mt9, 32));
  };

  WAITV(24);
  BARRIER();
  s_phase([&](int k) { if ((k & 3) == 1 && (k >> 2) < 8) pieceKV(vg + SLOT, false, 1 * SLOT, k >> 2); });
  NOPS();
#pragma unroll
  for (int k = 0; k < 15; ++k) read_scores(k);

#pragma unroll 1
  for (int t = 0; t < 8; ++t) {
    STAMP(15 + 2 * (t));
    WAITV(24);
    BARRIER();
    STAMP(16 + 2 * (t));
    if (__any((mt - m > THR) || (mt9 - m9 > THR))) {
      const float mn = fmaxf(m, mt), mn9 = fmaxf(m9, mt9);
      const float al = __builtin_amdgcn_exp2f((m - mn) * CS), al9 = __builtin_amdgcn_exp2f((m9 - mn9) * CS);
      m = mn; m9 = mn9; l *= al; l9 *= al9;
#pragma unroll
      for (int i = 0; i < 128; ++i) ASCALE(i, al);
#pragma unroll
      for (int i = 128; i < 192; ++i) ASCALE(i, al9);
    }
    float mc = m * CS, mc9 = m9 * CS;
    unsigned pk[18];
    float xa[2][2], ea[2][2], lp0 = 0.f, lp1 = 0.f;
    auto sm1 = [&](int pr) {
      const float c_ = pr < 16 ? mc : mc9;
      float s0 = sv[2 * pr], s1 = sv[2 * pr + 1];
      asm volatile("" : "+v"(s0), "+v"(s1));
      xa[pr & 1][0] = fmaf(s0, CS, -c_); xa[pr & 1][1] = fmaf(s1, CS, -c_);
    };
    auto sm2 = [&](int pr) {
      asm volatile("v_exp_f32 %0, %2\n\tv_exp_f32 %1, %3" : "=&v"(ea[pr & 1][0]), "=&v"(ea[pr & 1][1]) : "v"(xa[pr & 1][0]), "v"(xa[pr & 1][1]));
    };
    auto sm3 = [&](int pr) {
      const float e0 = ea[pr & 1][0], e1 = ea[pr & 1][1];
      asm volatile("v_cvt_pk_bf16_f32 %0, %1, %2" : "=v"(pk[pr]) : "v"(e0), "v"(e1));
      if (pr < 16) { asm volatile("v_add_f32 %0, %0, %2\n\tv_add_f32 %1, %1, %3" : "+v"(lp0), "+v"(lp1) : "v"(e0), "v"(e1)); }
      else l9 += e0 + e1;
    };
    {
      if (t == 5) {
#pragma unroll
        for (int n = 0; n < 8; ++n) voffK[n] = 0;
      }
      const int ps = sl == 0 ? 4 : sl - 1;
      const char* gk = kg + (size_t)(t < 5 ? t + 3 : 0) * SLOT;
      s_phase([&](int k) {
        if ((k & 1) == 0 && (k >> 1) < 18) sm1(k >> 1);
        if ((k & 1) == 1 && (k >> 1) < 18) sm2(k >> 1);
        if ((k & 1) == 0 && k >= 2 && (k >> 1) - 1 < 18) sm3((k >> 1) - 1);
        if ((k & 3) == 1 && (k >> 2) < 8) pieceKV(gk, true, ps * SLOT, k >> 2);
      });
      l += lp0 + lp1;
    }
#pragma unroll
    for (int i = 0; i < 4; ++i) {
      u32x4 wv = {pk[4 * i], pk[4 * i + 1], pk[4 * i + 2], pk[4 * i + 3]};
      pf[i] = __builtin_bit_cast(bf16x8, wv);
    }
    {
      u32x2 wv = {pk[16], pk[17]};
      p9f = __builtin_bit_cast(bf16x4, wv);
    }
    STAMP(31 + 2 * (t));
    WAITV(24);
    BARRIER();
    STAMP(32 + 2 * (t));
    {
      bf16x8 fr[12];
      unsigned bV0 = lds_base + sl * SLOT + laneV; OPAQUE(bV0);
      const unsigned bV1 = (bV0 ^ 32) + 4096;
      unsigned bV9 = lds_base + sl * SLOT + laneV9; OPAQUE(bV9);
      auto ldf = [&](int k) -> bf16x8 {
        if (k < 32) {
          const int kp = k >> 3, d = k & 7;
          return trA2((bV0 ^ ((d & 3) << 6)) + (d >> 2) * 256 + kp * 8192, (bV1 ^ ((d & 3) << 6)) + (d >> 2) * 256 + kp * 8192);
        } else {
          const int d = k - 32;
          return trA2((bV9 ^ ((d & 3) << 6)) + (d >> 2) * 256, ((bV9 ^ 32) ^ ((d & 3) << 6)) + (d >> 2) * 256);
        }
      };
#pragma unroll
      for (int k = 0; k < 8; ++k) { const int kk = (k & ~3) + (3 - (k & 3)); fr[kk] = ldf(kk); }
      if (t == 6) {
#pragma unroll
        for (int n = 0; n < 8; ++n) voffV[n] = 0;
      }
      const int ps = sl == 0 ? 4 : sl - 1;
      const char* gv = vg + (size_t)(t < 6 ? t + 2 : 0) * SLOT;
      SB();
#pragma unroll
      for (int k = 0; k < 40; ++k) {
        if (k < 32) M32(16 * (k & 7), fr[k % 12], pf[k >> 3]);
        else {
          const int d = k - 32;
          bf16x4 lo = __builtin_shufflevector(fr[k % 12], fr[k % 12], 0, 1, 2, 3), hi4 = __builtin_shufflevector(fr[k % 12], fr[k % 12], 4, 5, 6, 7);
          M16x16(128 + 8 * d, lo, p9f);
          M16x16(132 + 8 * d, hi4, p9f);
        }
        { const int kk = (k & ~3) + 8 + (3 - (k & 3)); if (kk < 40) fr[kk % 12] = ldf(kk); }
        if ((k & 3) == 1 && (k >> 2) < 8) pieceKV(gv, false, ps * SLOT, k >> 2);
        if (k >= 2 && k < 17) read_scores(k - 2);
        SB();
      }
      sl = sl == 4 ? 0 : sl + 1;
    }
  }
  WAITV(0);
  NOPS();
  l += __shfl_xor(l, 32);
  l9 += __shfl_xor(l9, 16);
  l9 += __shfl_xor(l9, 32);
  const float inv_l = 1.0f / l;

  STAMP(47);
  char* ofp = (char*)Of + (size_t)p * OF_PER_P * 2;
  {
    char* dst = ofp + ((size_t)(4 * b + w) * 256 + h * 16) * 1024 + lane * 16;
#pragma unroll
    for (int s = 0; s < 16; ++s) {
      const int A = 16 * (s >> 1) + 8 * (s & 1);
      bf16x8 v = pack8(AREAD(A) * inv_l, AREAD(A + 1) * inv_l, AREAD(A + 2) * inv_l, AREAD(A + 3) * inv_l,
                       AREAD(A + 4) * inv_l, AREAD(A + 5) * inv_l, AREAD(A + 6) * inv_l, AREAD(A + 7) * inv_l);
      *reinterpret_cast<bf16x8*>(dst + s * 1024) = v;
    }
  }
  RELANE();
  BARRIER();
  {
    float* st = reinterpret_cast<float*>(smem + 4 * 16 * 1040);
#pragma unroll
    for (int d = 0; d < 16; ++d) {
      f32x4 v = {AREAD(128 + 4 * d), AREAD(129 + 4 * d), AREAD(130 + 4 * d), AREAD(131 + 4 * d)};
      *reinterpret_cast<f32x4*>(smem + (w * 16 + i16) * 1040 + (16 * d + 4 * g) * 4) = v;
    }
    if (g == 0) { st[(w * 16 + i16) * 2] = m9 * CS; st[(w * 16 + i16) * 2 + 1] = l9; }
    BARRIER();
#pragma unroll
    for (int it = 0; it < 2; ++it) {
      const int item = tid + 256 * it;
      const int q = item >> 5, ck = item & 31;
      float mw[4], lw[4];
#pragma unroll
      for (int ww = 0; ww < 4; ++ww) { mw[ww] = st[(ww * 16 + q) * 2]; lw[ww] = st[(ww * 16 + q) * 2 + 1]; }
      const float M = fmaxf(fmaxf(mw[0], mw[1]), fmaxf(mw[2], mw[3]));
      float f[4], L = 0.f;
#pragma unroll
      for (int ww = 0; ww < 4; ++ww) { f[ww] = __builtin_amdgcn_exp2f(mw[ww] - M); L += f[ww] * lw[ww]; }
      const float il = 1.0f / L;
      float o[8];
#pragma unroll
      for (int j = 0; j < 8; ++j) o[j] = 0.f;
#pragma unroll
      for (int ww = 0; ww < 4; ++ww) {
        const f32x4 a = *reinterpret_cast<const f32x4*>(smem + (ww * 16 + q) * 1040 + ck * 32);
        const f32x4 c2 = *reinterpret_cast<const f32x4*>(smem + (ww * 16 + q) * 1040 + ck * 32 + 16);
#pragma unroll
        for (int j = 0; j < 4; ++j) { o[j] += f[ww] * a[j]; o[4 + j] += f[ww] * c2[j]; }
      }
      bf16x8 v = pack8(o[0] * il, o[1] * il, o[2] * il, o[3] * il, o[4] * il, o[5] * il, o[6] * il, o[7] * il);
      char* dst = ofp + ((size_t)(32 + (b >> 1)) * 256 + h * 16 + (ck >> 1)) * 1024 + (16 * (b & 1) + q + 32 * (ck & 1)) * 16;
      *reinterpret_cast<bf16x8*>(dst) = v;
    }
  }
  STAMP(48);
}

constexpr int K2_NBUF = 5;
constexpr int K2_WAVE_LDS = K2_NBUF * 4096;
DI bf16x8 gload16_asm(const char* p) {
  bf16x8 r;
  asm volatile("global_load_dwordx4 %0, %1, off" : "=v"(r) : "v"(p) : "memory");
  return r;
}
__global__ __launch_bounds__(512, 2) void oproj_kernel(const __hip_bfloat16* __restrict__ Of, const __hip_bfloat16* __restrict__ woA,
                                                       const __hip_bfloat16* __restrict__ woB, __hip_bfloat16* __restrict__ out) {
  extern __shared__ __attribute__((aligned(16))) char smem[];
  const int tid = threadIdx.x, lane = tid & 63;
  const int w = __builtin_amdgcn_readfirstlane(tid >> 6);
  const int T = (blockIdx.x & 7) * 32 + (blockIdx.x >> 3);
  if (T >= 252) return;
  const int p = T / 126, rem = T % 126, mg = rem / 7, ng = rem % 7;
  const char* of = (const char*)Of + (size_t)p * OF_PER_P * 2 + ((size_t)(2 * mg) * 256 + w * 32) * 1024 + lane * 16;
  const char* wo = (const char*)(p ? woB : woA) + (size_t)(w * 512) * (DM * 2) + ng * 128;
  const int hi = lane >> 5, blk = (lane >> 4) & 1, q4 = (lane & 15) >> 2, pp = lane & 3;
  const unsigned lds_base = (unsigned)(size_t)(lds_b4_ptr)smem + w * K2_WAVE_LDS;
  const unsigned voff = (unsigned)((lane >> 3) * (DM * 2) + (((lane & 7) ^ (((lane >> 4) & 1) << 2)) << 4));

  bf16x8 ob[K2_NBUF][2][2];
  auto issue = [&](int ck) {
#pragma unroll
    for (int mt = 0; mt < 2; ++mt)
#pragma unroll
      for (int ks = 0; ks < 2; ++ks) ob[ck % K2_NBUF][mt][ks] = gload16_asm(of + ((size_t)mt * 256 + 2 * ck + ks) * 1024);
#pragma unroll
    for (int n = 0; n < 4; ++n)
      glds16(wo + (size_t)(32 * ck + 8 * n) * (DM * 2), voff, lds_base + (ck % K2_NBUF) * 4096 + n * 1024);
  };
  f32x16 acc[2][2];
#pragma unroll
  for (int a = 0; a < 2; ++a)
#pragma unroll
    for (int c = 0; c < 2; ++c) acc[a][c] = f32x16{};
  issue(0); issue(1); issue(2); issue(3);
#pragma unroll
  for (int ck = 0; ck < 16; ++ck) {
    if (ck + 4 < 16) { issue(ck + 4); WAITV(32); }
    else if (ck == 12) WAITV(24); else if (ck == 13) WAITV(16); else if (ck == 14) WAITV(8); else WAITV(0);
    {
      bf16x8 (&o)[2][2] = ob[ck % K2_NBUF];
      asm volatile("" : "+v"(o[0][0]), "+v"(o[0][1]), "+v"(o[1][0]), "+v"(o[1][1]));
    }
    const unsigned sb = w * K2_WAVE_LDS + (ck % K2_NBUF) * 4096;
    bf16x8 fa[2][2];
#pragma unroll
    for (int ks = 0; ks < 2; ++ks)
#pragma unroll
      for (int nt = 0; nt < 2; ++nt) {
        const int row0 = 16 * ks + 8 * hi + q4, row1 = row0 + 4;
        const int ch = 4 * nt + 2 * blk + (pp >> 1);
        fa[ks][nt] = tr_read2(smem, sb + row0 * 128 + ((ch ^ (((row0 >> 1) & 1) << 2)) << 4) + 8 * (pp & 1),
                              sb + row1 * 128 + ((ch ^ (((row1 >> 1) & 1) << 2)) << 4) + 8 * (pp & 1));
      }
#pragma unroll
    for (int ks = 0; ks < 2; ++ks)
#pragma unroll
      for (int nt = 0; nt < 2; ++nt)
#pragma unroll
        for (int mt = 0; mt < 2; ++mt) acc[nt][mt] = mfma32(fa[ks][nt], ob[ck % K2_NBUF][mt][ks], acc[nt][mt]);
    asm volatile("s_waitcnt lgkmcnt(0)" ::: "memory");
  }
  __syncthreads();
  const int r32 = lane & 31;
#pragma unroll
  for (int nt = 0; nt < 2; ++nt)
#pragma unroll
    for (int mt = 0; mt < 2; ++mt)
#pragma unroll
      for (int qd = 0; qd < 4; ++qd) {
        f32x4 v = {acc[nt][mt][4 * qd], acc[nt][mt][4 * qd + 1], acc[nt][mt][4 * qd + 2], acc[nt][mt][4 * qd + 3]};
        *reinterpret_cast<f32x4*>(smem + ((size_t)w * 64 + 32 * mt + r32) * 272 + (32 * nt + 8 * qd + 4 * hi) * 4) = v;
      }
  __syncthreads();
  {
    const int m = tid >> 3, nc = tid & 7;
    float o[8];
#pragma unroll
    for (int j = 0; j < 8; ++j) o[j] = 0.f;
#pragma unroll
    for (int ww = 0; ww < 8; ++ww) {
      const f32x4 a = *reinterpret_cast<const f32x4*>(smem + ((size_t)ww * 64 + m) * 272 + nc * 32);
      const f32x4 c = *reinterpret_cast<const f32x4*>(smem + ((size_t)ww * 64 + m) * 272 + nc * 32 + 16);
#pragma unroll
      for (int j = 0; j < 4; ++j) { o[j] += a[j]; o[4 + j] += c[j]; }
    }
    const int mt = 2 * mg + (m >> 5), r = m & 31;
    int row;
    if (mt < 32) row = (mt >> 2) * SQ + (mt & 3) * 32 + r;
    else row = (2 * (mt - 32) + (r >> 4)) * SQ + 128 + (r & 15);
    bf16x8 v = pack8(o[0], o[1], o[2], o[3], o[4], o[5], o[6], o[7]);
    *reinterpret_cast<bf16x8*>((char*)out + ((size_t)row * (2 * DM) + p * DM + ng * 64 + nc * 8) * 2) = v;
  }
}

extern "C" void kernel_launch(void* const* d_in, const int* in_sizes, int n_in, void* d_out, int out_size, void* d_ws,
                              size_t ws_size, hipStream_t stream) {
  const __hip_bfloat16* xA = (const __hip_bfloat16*)d_in[0];
  const __hip_bfloat16* kA = (const __hip_bfloat16*)d_in[1];
  const __hip_bfloat16* vA = (const __hip_bfloat16*)d_in[2];
  const __hip_bfloat16* wqA = (const __hip_bfloat16*)d_in[3];
  const __hip_bfloat16* woA = (const __hip_bfloat16*)d_in[4];
  const __hip_bfloat16* xB = (const __hip_bfloat16*)d_in[5];
  const __hip_bfloat16* kB = (const __hip_bfloat16*)d_in[6];
  const __hip_bfloat16* vB = (const __hip_bfloat16*)d_in[7];
  const __hip_bfloat16* wqB = (const __hip_bfloat16*)d_in[8];
  const __hip_bfloat16* woB = (const __hip_bfloat16*)d_in[9];
  __hip_bfloat16* Of = (__hip_bfloat16*)d_ws;
  hipFuncSetAttribute((const void*)attn_kernel, hipFuncAttributeMaxDynamicSharedMemorySize, LDS_BYTES);
  hipFuncSetAttribute((const void*)oproj_kernel, hipFuncAttributeMaxDynamicSharedMemorySize, 8 * K2_WAVE_LDS);
  attn_kernel<<<256, 256, LDS_BYTES, stream>>>(xA, kA, vA, wqA, xB, kB, vB, wqB, Of, woA, woB
  );
  oproj_kernel<<<256, 512, 8 * K2_WAVE_LDS, stream>>>(Of, woA, woB, (__hip_bfloat16*)d_out);
}
```

```cpp
#include <hip/hip_runtime.h>
#include <hip/hip_bf16.h>
#include <stdint.h>
#include <type_traits>
#define ILQ 2
#define ILF 4

typedef short bf16x8 __attribute__((ext_vector_type(8)));
typedef short bf16x4 __attribute__((ext_vector_type(4)));
typedef float f32x16 __attribute__((ext_vector_type(16)));
typedef float f32x4 __attribute__((ext_vector_type(4)));
typedef unsigned u32x4 __attribute__((ext_vector_type(4)));
typedef unsigned u32x2 __attribute__((ext_vector_type(2)));
typedef __bf16 bf2_t __attribute__((ext_vector_type(2)));
typedef __attribute__((address_space(3))) bf16x4* lds_b4_ptr;

#define DI __device__ __forceinline__

constexpr int NB = 8, SQ = 144, DM = 448, NH = 16, SK = 512, DH = 256;
constexpr int SLOT = 32768;
constexpr int LDS_BYTES = 5 * SLOT;
constexpr size_t OF_PER_P = (size_t)36 * 256 * 512;

DI unsigned pack2(float a, float b) {
  bf2_t v = {(__bf16)a, (__bf16)b};
  return __builtin_bit_cast(unsigned, v);
}
DI bf16x8 pack8(float a0, float a1, float a2, float a3, float a4, float a5, float a6, float a7) {
  u32x4 w = {pack2(a0, a1), pack2(a2, a3), pack2(a4, a5), pack2(a6, a7)};
  return __builtin_bit_cast(bf16x8, w);
}
DI f32x16 mfma32(bf16x8 a, bf16x8 b, f32x16 c) { return __builtin_amdgcn_mfma_f32_32x32x16_bf16(a, b, c, 0, 0, 0); }
DI f32x4 mfma16x32(bf16x8 a, bf16x8 b, f32x4 c) { return __builtin_amdgcn_mfma_f32_16x16x32_bf16(a, b, c, 0, 0, 0); }
DI f32x4 mfma16x16(bf16x4 a, bf16x4 b, f32x4 c) { return __builtin_amdgcn_mfma_f32_16x16x16bf16_1k(a, b, c, 0, 0, 0); }

#define WAITV(n) asm volatile("s_waitcnt vmcnt(%0)" ::"n"(n) : "memory")
#define SB() __builtin_amdgcn_sched_barrier(0)
#define STAMP(i)
#define BARRIER() do { asm volatile("s_waitcnt lgkmcnt(0)" ::: "memory"); __builtin_amdgcn_s_barrier(); asm volatile("" ::: "memory"); } while (0)

DI bf16x4 tr_read(const char* smem, unsigned off) {
  return __builtin_amdgcn_ds_read_tr16_b64_v4i16((lds_b4_ptr)(smem + off));
}
DI bf16x8 tr_read2(const char* smem, unsigned off0, unsigned off1) {
  bf16x4 lo = tr_read(smem, off0), hi = tr_read(smem, off1);
  return __builtin_shufflevector(lo, hi, 0, 1, 2, 3, 4, 5, 6, 7);
}
DI void glds16(const char* gbase, unsigned voff, unsigned ldst) {
  unsigned keep;
  asm volatile("s_mov_b32 %0, m0\n\ts_mov_b32 m0, %3\n\ts_nop 0\n\tglobal_load_lds_dwordx4 %1, %2\n\ts_mov_b32 m0, %0"
               : "=&s"(keep) : "v"(voff), "s"(gbase), "s"(ldst) : "memory");
}
DI void glds16nt(const char* gbase, unsigned voff, unsigned ldst) {
  unsigned keep;
  asm volatile("s_mov_b32 %0, m0\n\ts_mov_b32 m0, %3\n\ts_nop 0\n\tglobal_load_lds_dwordx4 %1, %2 nt\n\ts_mov_b32 m0, %0"
               : "=&s"(keep) : "v"(voff), "s"(gbase), "s"(ldst) : "memory");
}
DI bf16x8 lds128(const char* smem, unsigned off) { return *reinterpret_cast<const bf16x8*>(smem + off); }

typedef __attribute__((address_space(3))) bf16x8* lds_b8_ptr;
DI bf16x4 trA(unsigned addr) { return __builtin_amdgcn_ds_read_tr16_b64_v4i16(reinterpret_cast<lds_b4_ptr>(addr)); }
DI bf16x8 trA2(unsigned a0, unsigned a1) {
  bf16x4 lo = trA(a0), hi = trA(a1);
  return __builtin_shufflevector(lo, hi, 0, 1, 2, 3, 4, 5, 6, 7);
}
DI bf16x8 ldA(unsigned addr) { return *reinterpret_cast<lds_b8_ptr>(addr); }
DI void ascale(float& a, float s) {
  float tmp;
  asm volatile("v_accvgpr_read_b32 %1, %0\n\tv_mul_f32 %1, %1, %2\n\tv_accvgpr_write_b32 %0, %1" : "+a"(a), "=&v"(tmp) : "v"(s));
}
DI void amfma32(f32x16& c, bf16x8 a, bf16x8 b) { asm volatile("v_mfma_f32_32x32x16_bf16 %0, %1, %2, %0" : "+a"(c) : "v"(a), "v"(b)); }
DI void amfma32z(f32x16& c, bf16x8 a, bf16x8 b) { asm volatile("v_mfma_f32_32x32x16_bf16 %0, %1, %2, 0" : "=a"(c) : "v"(a), "v"(b)); }
DI void amfma16x32(f32x4& c, bf16x8 a, bf16x8 b) { asm volatile("v_mfma_f32_16x16x32_bf16 %0, %1, %2, %0" : "+a"(c) : "v"(a), "v"(b)); }
DI void amfma16x32z(f32x4& c, bf16x8 a, bf16x8 b) { asm volatile("v_mfma_f32_16x16x32_bf16 %0, %1, %2, 0" : "=a"(c) : "v"(a), "v"(b)); }
DI void amfma16x16(f32x4& c, bf16x4 a, bf16x4 b) { asm volatile("v_mfma_f32_16x16x16_bf16 %0, %1, %2, %0" : "+a"(c) : "v"(a), "v"(b)); }
#define MFMA_DRAIN() asm volatile("s_nop 15\n\ts_nop 15" ::: "memory")
#define OPAQUE(x) asm volatile("" : "+v"(x))

DI int swzT(int row) { return ((row & 3) << 2) | ((((row >> 2) ^ (row >> 3)) & 1) << 1); }
DI int swap2(int p) { return ((p & 1) << 1) | (p >> 1); }

constexpr int STG = 53248;

#define ALL_AGPRS "a0", "a1", "a2", "a3", "a4", "a5", "a6", "a7", "a8", "a9", "a10", "a11", "a12", "a13", "a14", "a15", "a16", "a17", "a18", "a19", "a20", "a21", "a22", "a23", "a24", "a25", "a26", "a27", "a28", "a29", "a30", "a31", "a32", "a33", "a34", "a35", "a36", "a37", "a38", "a39", "a40", "a41", "a42", "a43", "a44", "a45", "a46", "a47", "a48", "a49", "a50", "a51", "a52", "a53", "a54", "a55", "a56", "a57", "a58", "a59", "a60", "a61", "a62", "a63", "a64", "a65", "a66", "a67", "a68", "a69", "a70", "a71", "a72", "a73", "a74", "a75", "a76", "a77", "a78", "a79", "a80", "a81", "a82", "a83", "a84", "a85", "a86", "a87", "a88", "a89", "a90", "a91", "a92", "a93", "a94", "a95", "a96", "a97", "a98", "a99", "a100", "a101", "a102", "a103", "a104", "a105", "a106", "a107", "a108", "a109", "a110", "a111", "a112", "a113", "a114", "a115", "a116", "a117", "a118", "a119", "a120", "a121", "a122", "a123", "a124", "a125", "a126", "a127", "a128", "a129", "a130", "a131", "a132", "a133", "a134", "a135", "a136", "a137", "a138", "a139", "a140", "a141", "a142", "a143", "a144", "a145", "a146", "a147", "a148", "a149", "a150", "a151", "a152", "a153", "a154", "a155", "a156", "a157", "a158", "a159", "a160", "a161", "a162", "a163", "a164", "a165", "a166", "a167", "a168", "a169", "a170", "a171", "a172", "a173", "a174", "a175", "a176", "a177", "a178", "a179", "a180", "a181", "a182", "a183", "a184", "a185", "a186", "a187", "a188", "a189", "a190", "a191", "a192", "a193", "a194", "a195", "a196", "a197", "a198", "a199", "a200", "a201", "a202", "a203", "a204", "a205", "a206", "a207", "a208", "a209", "a210", "a211", "a212", "a213", "a214", "a215", "a216", "a217", "a218", "a219", "a220", "a221", "a222", "a223", "a224", "a225", "a226", "a227", "a228", "a229", "a230", "a231", "a232", "a233", "a234", "a235", "a236", "a237", "a238", "a239", "a240", "a241", "a242", "a243", "a244", "a245", "a246", "a247", "a248", "a249", "a250", "a251", "a252", "a253", "a254", "a255"
DI void M32(int A, bf16x8 a, bf16x8 b) { asm volatile("v_mfma_f32_32x32x16_bf16 a[%c2:%c3], %0, %1, a[%c2:%c3]" ::"v"(a), "v"(b), "i"(A), "i"(A + 15) : ALL_AGPRS); }
DI void M32Z(int A, bf16x8 a, bf16x8 b) { asm volatile("v_mfma_f32_32x32x16_bf16 a[%c2:%c3], %0, %1, 0" ::"v"(a), "v"(b), "i"(A), "i"(A + 15) : ALL_AGPRS); }
DI void M16x32(int A, bf16x8 a, bf16x8 b) { asm volatile("v_mfma_f32_16x16x32_bf16 a[%c2:%c3], %0, %1, a[%c2:%c3]" ::"v"(a), "v"(b), "i"(A), "i"(A + 3) : ALL_AGPRS); }
DI void M16x32Z(int A, bf16x8 a, bf16x8 b) { asm volatile("v_mfma_f32_16x16x32_bf16 a[%c2:%c3], %0, %1, 0" ::"v"(a), "v"(b), "i"(A), "i"(A + 3) : ALL_AGPRS); }
DI void M16x16(int A, bf16x4 a, bf16x4 b) { asm volatile("v_mfma_f32_16x16x16_bf16 a[%c2:%c3], %0, %1, a[%c2:%c3]" ::"v"(a), "v"(b), "i"(A), "i"(A + 3) : ALL_AGPRS); }
DI float AREAD(int A) { float r; asm volatile("v_accvgpr_read_b32 %0, a%c1" : "=v"(r) : "i"(A)); return r; }
DI void AZERO(int A) { asm volatile("v_accvgpr_write_b32 a%c0, 0" ::"i"(A) : ALL_AGPRS); }
DI void ASCALE(int A, float s) {
  float t;
  asm volatile("v_accvgpr_read_b32 %0, a%c2\n\tv_mul_f32 %0, %0, %1\n\tv_accvgpr_write_b32 a%c2, %0" : "=&v"(t) : "v"(s), "i"(A));
}
#define NOPS() asm volatile("s_nop 15\n\ts_nop 15")
template <int OFF, bool NT>
DI void dma(const char* gbase, unsigned voff, unsigned ldst) {
  if (NT) asm volatile("s_mov_b32 m0, %2\n\ts_nop 0\n\tglobal_load_lds_dwordx4 %0, %1 offset:%c3 nt" ::"v"(voff), "s"(gbase), "s"(ldst), "i"(OFF) : "memory");
  else asm volatile("s_mov_b32 m0, %2\n\ts_nop 0\n\tglobal_load_lds_dwordx4 %0, %1 offset:%c3" ::"v"(voff), "s"(gbase), "s"(ldst), "i"(OFF) : "memory");
}

template <bool NT>
DI void dmap(const char* gbase, unsigned voff, unsigned ldst, int flag) {
  asm volatile("s_cmp_eq_u32 %3, 0\n\ts_cbranch_scc1 1f\n\ts_mov_b32 m0, %2\n\ts_nop 0\n\tglobal_load_lds_dwordx4 %0, %1\n1:"
               ::"v"(voff), "s"(gbase), "s"(ldst), "s"(flag) : "memory", "scc");
}
DI void zero_acc() {
  bf16x8 z = {};
  asm volatile("" : "+v"(z));
#pragma unroll
  for (int i = 0; i < 12; ++i) M32Z(16 * i, z, z);
}

__global__ __launch_bounds__(256, 1) void attn_kernel(
    const __hip_bfloat16* __restrict__ xA, const __hip_bfloat16* __restrict__ kA, const __hip_bfloat16* __restrict__ vA,
    const __hip_bfloat16* __restrict__ wqA, const __hip_bfloat16* __restrict__ xB, const __hip_bfloat16* __restrict__ kB,
    const __hip_bfloat16* __restrict__ vB, const __hip_bfloat16* __restrict__ wqB, __hip_bfloat16* __restrict__ Of,
    const __hip_bfloat16* __restrict__ woA, const __hip_bfloat16* __restrict__ woB
    ) {
  extern __shared__ __attribute__((aligned(16))) char smem[];
  asm volatile("" ::: "a0", "a64", "a128", "a192", "a227");
  const int tid = threadIdx.x, lane = tid & 63;
  const int w = __builtin_amdgcn_readfirstlane(tid >> 6);
  const int bid = blockIdx.x, xcd = bid & 7, idx = bid >> 3;
  const int ph = xcd * 4 + (idx >> 3), p = ph >> 4, h = ph & 15, b = idx & 7;
  const char* xg = (const char*)(p ? xB : xA) + (size_t)b * SQ * DM * 2;
  const char* wg = (const char*)(p ? wqB : wqA) + (size_t)h * DH * 2;
  const char* kg = (const char*)(p ? kB : kA) + (size_t)(b * NH + h) * SK * DH * 2;
  const char* vg = (const char*)(p ? vB : vA) + (size_t)(b * NH + h) * SK * DH * 2;

  int r32 = lane & 31, hi = lane >> 5, i16 = lane & 15, g = lane >> 4;
  int blk = (lane >> 4) & 1, q4 = (lane & 15) >> 2, pp = lane & 3;
#define RELANE() do { int l_ = lane; asm volatile("" : "+v"(l_)); r32 = l_ & 31; hi = l_ >> 5; i16 = l_ & 15; g = l_ >> 4; \
    blk = (l_ >> 4) & 1; q4 = (l_ & 15) >> 2; pp = l_ & 3; } while (0)

  const unsigned lds_base = (unsigned)(size_t)(lds_b4_ptr)smem;
  unsigned voffK[8], voffV[8], voffW[8], voffX[5];
#pragma unroll
  for (int n = 0; n < 8; ++n) {
    const unsigned xk = (unsigned)(2 * n), xv = (unsigned)((((2 * n) & 3) << 2) | ((((n >> 1) ^ (n >> 2)) & 1) << 1));
    voffK[n] = (unsigned)((16 * w + hi) * 512 + (((r32 ^ hi) ^ xk) << 4));
    voffV[n] = (unsigned)((16 * w + hi) * 512 + (((r32 ^ (hi << 2)) ^ xv) << 4));
    voffW[n] = (unsigned)((16 * w + hi + 2 * n) * 8192 + (((r32 ^ (hi << 2)) ^ xv) << 4));
  }
#pragma unroll
  for (int n = 0; n < 5; ++n) {
    const int gi = 5 * w + n;
    int row = 8 * gi + (lane >> 3);
    const int ch = (lane & 7) ^ ((row >> 1) & 7);
    row = row < SQ ? row : SQ - 1;
    voffX[n] = (unsigned)(row * (DM * 2) + ch * 16);
  }
  const unsigned ldsw = lds_base + w * 8192;
  auto pieceKV = [&](const char* gb, bool isK, unsigned ldst, int n) {
    const unsigned vo = isK ? voffK[n] : voffV[n];
    const char* g2 = gb + (n >> 2) * 4096;
    const unsigned l2 = ldsw + ldst + (n >> 2) * 4096;
    switch (n & 3) {
      case 0: dma<0, true>(g2, vo, l2); break;
      case 1: dma<1024, true>(g2, vo, l2); break;
      case 2: dma<2048, true>(g2, vo, l2); break;
      default: dma<3072, true>(g2, vo, l2); break;
    }
  };
  auto pieceQ = [&](const char* wgc, const char* xgc, unsigned ldst, int n) {
    if (n < 8) dma<0, false>(wgc, voffW[n], ldsw + ldst + n * 1024);
    else dma<0, false>(xgc, voffX[n - 8], lds_base + ldst + SLOT + (5 * w + n - 8) * 1024);
  };
  STAMP(0);
  {
    const char* wop = (const char*)(p ? woB : woA) + (size_t)(b * NH + h) * 28672 + w * 7168;
#pragma unroll
    for (int n = 0; n < 7; ++n) dma<0, false>(wop + n * 1024, (unsigned)(lane * 16), lds_base + 3 * STG + w * 1024);
  }
#pragma unroll
  for (int n = 0; n < 13; ++n) pieceQ(wg, xg, 0, n);
#pragma unroll
  for (int n = 0; n < 13; ++n) pieceQ(wg + 64 * NH * DH * 2, xg + 128, STG, n);
  zero_acc();

  {
    const int spw = swap2(pp);
    const unsigned laneW = (unsigned)((8 * hi + q4) * 512 + ((((q4 << 2) | ((blk ^ hi) << 1)) ^ (spw >> 1)) << 4) + 8 * (spw & 1));
    const unsigned lane9 = (unsigned)((8 * g + q4) * 512 + (((8 * w + (pp >> 1)) ^ ((q4 << 2) | ((g & 1) << 1))) << 4) + 8 * (pp & 1));
    const unsigned laneX = (unsigned)(SLOT + (32 * w + r32) * 128 + ((hi ^ ((r32 >> 1) & 7)) << 4));
    const unsigned laneX9 = (unsigned)(SLOT + (128 + i16) * 128 + ((g ^ ((i16 >> 1) & 7)) << 4));
    unsigned stg = 0;
#pragma unroll 1
    for (int c = 0; c < 7; ++c) {
      STAMP(1 + 2 * c);
      if (c < 6) WAITV(13); else WAITV(8);
      BARRIER();
      STAMP(2 + 2 * c);
      bf16x8 fr[12], xq[4], x9q[2];
      unsigned bW0 = lds_base + stg + laneW; OPAQUE(bW0);
      unsigned bX = lds_base + stg + laneX; OPAQUE(bX);
      unsigned bX9 = lds_base + stg + laneX9; OPAQUE(bX9);
      const unsigned bW1 = (bW0 ^ 32) + 2048;
      unsigned b9 = lds_base + stg + lane9; OPAQUE(b9);
      auto ldf = [&](int k) -> bf16x8 {
        if (k < 32) {
          const int ks = k >> 3, d = k & 7;
          return trA2((bW0 ^ ((d & 3) << 6)) + (d >> 2) * 256 + ks * 8192, (bW1 ^ ((d & 3) << 6)) + (d >> 2) * 256 + ks * 8192);
        } else {
          const int ks = (k - 32) >> 2, d = (k - 32) & 3;
          return trA2((b9 ^ (d << 5)) + ks * 16384, ((b9 ^ 32) ^ (d << 5)) + 2048 + ks * 16384);
        }
      };
#pragma unroll
      for (int ks = 0; ks < 4; ++ks) xq[ks] = ldA(bX ^ (ks << 5));
#pragma unroll
      for (int ks = 0; ks < 2; ++ks) x9q[ks] = ldA(bX9 ^ (ks << 6));
#pragma unroll
      for (int k = 0; k < 8; ++k) { const int kk = (k & ~3) + (3 - (k & 3)); fr[kk] = ldf(kk); }
      unsigned nx = stg + 2 * STG; nx = nx >= 3 * STG ? nx - 3 * STG : nx;
      const char* wgc = wg + (size_t)(c + 2) * (64 * NH * DH * 2);
      const char* xgc = xg + (c + 2) * 128;
      if (c == 5) {
#pragma unroll
        for (int n = 0; n < 8; ++n) pieceKV(kg, true, 2 * SLOT, n);
      } else if (c == 6) {
#pragma unroll
        for (int n = 0; n < 8; ++n) pieceKV(kg + SLOT, true, 3 * SLOT, n);
#pragma unroll
        for (int n = 0; n < 8; ++n) pieceKV(vg, false, 4 * SLOT, n);
      }
      const int fq = __builtin_amdgcn_readfirstlane(c <= 4);
      SB();
#pragma unroll
      for (int k = 0; k < 40; ++k) {
        if (k < 32) M32(16 * (k & 7), fr[k % 12], xq[k >> 3]);
        else M16x32(128 + 4 * ((k - 32) & 3), fr[k % 12], x9q[(k - 32) >> 2]);
        { const int kk = (k & ~3) + 8 + (3 - (k & 3)); if (kk < 40) fr[kk % 12] = ldf(kk); }
        if ((k % 3) == 0 && k / 3 < 13) {
          const int n = k / 3;
          if (n < 8) dmap<false>(wgc, voffW[n], ldsw + nx + n * 1024, fq);
          else dmap<false>(xgc, voffX[n - 8], lds_base + nx + SLOT + (5 * w + n - 8) * 1024, fq);
        }
        SB();
      }
      stg += STG; stg = stg >= 3 * STG ? 0 : stg;
    }
  }
  NOPS();
  bf16x8 Qf[16];
#pragma unroll
  for (int s = 0; s < 16; ++s) {
    const int A = 16 * (s >> 1) + 8 * (s & 1);
    Qf[s] = pack8(AREAD(A), AREAD(A + 1), AREAD(A + 2), AREAD(A + 3), AREAD(A + 4), AREAD(A + 5), AREAD(A + 6), AREAD(A + 7));
  }
  RELANE();
  BARRIER();
#pragma unroll
  for (int d = 0; d < 4; ++d) {
    u32x2 v = {pack2(AREAD(128 + 4 * d), AREAD(129 + 4 * d)), pack2(AREAD(130 + 4 * d), AREAD(131 + 4 * d))};
    const int dh = 64 * w + 16 * d + 4 * g;
    *reinterpret_cast<u32x2*>(smem + SLOT + i16 * 512 + (((dh >> 3) ^ i16) << 4) + ((dh & 4) << 1)) = v;
  }
  zero_acc();
  bf16x8 Q9f[8];
  BARRIER();
#pragma unroll
  for (int s = 0; s < 8; ++s) Q9f[s] = ldA(lds_base + SLOT + i16 * 512 + (((4 * s + g) ^ i16) << 4));

  constexpr float CS = 0.0625f * 1.4426950408889634f;
  constexpr float THR = 8.0f / CS;
  float m = -3.0e38f, l = 0.f, m9 = -3.0e38f, l9 = 0.f;
  const unsigned laneK = (unsigned)(r32 * 512 + ((hi ^ (r32 & 15)) << 4));
  const unsigned laneK9 = (unsigned)((16 * w + i16) * 512 + ((g ^ i16) << 4));
  const int spv = swap2(pp);
  const unsigned laneV = (unsigned)((4 * hi + q4) * 512 + ((((q4 << 2) | ((blk ^ hi) << 1)) ^ (spv >> 1)) << 4) + 8 * (spv & 1));
  const unsigned laneV9 = (unsigned)((16 * w + 4 * g + q4) * 512 + (((pp >> 1) ^ ((q4 << 2) | (((g ^ (g >> 1)) & 1) << 1))) << 4) + 8 * (pp & 1));
#define NEXTSL(x) ((x) == 4 ? 1 : (x) == 1 ? 0 : (x) == 0 ? 2 : (x) + 1)
#define PREVSL(x) ((x) == 1 ? 4 : (x) == 0 ? 1 : (x) == 2 ? 0 : (x) - 1)
  int sl = 2;
  float sv[36];
  float mt, mt9;
  bf16x8 pf[4];
  bf16x4 p9f;

  auto s_phase = [&](auto&& extra) {
    bf16x8 fr[12];
    unsigned bK = lds_base + sl * SLOT + laneK; OPAQUE(bK);
    unsigned bK9 = lds_base + sl * SLOT + laneK9; OPAQUE(bK9);
    auto ldf = [&](int k) -> bf16x8 {
      if (k < 32) return ldA((bK ^ ((k & 7) << 5)) + ((k >> 3) & 1) * 256 + (k >> 4) * 16384);
      else return ldA((bK9 ^ ((k & 3) << 6)) + ((k >> 2) & 1) * 256);
    };
#pragma unroll
    for (int k = 0; k < 8; ++k) { const int kk = (k & ~3) + (3 - (k & 3)); fr[kk] = ldf(kk); }
    SB();
#pragma unroll
    for (int k = 0; k < 40; ++k) {
      if (k < 32) { if ((k & 15) == 0) M32Z(192 + 16 * (k >> 4), fr[k % 12], Qf[k & 15]); else M32(192 + 16 * (k >> 4), fr[k % 12], Qf[k & 15]); }
      else { if (k == 32) M16x32Z(224, fr[k % 12], Q9f[k - 32]); else M16x32(224, fr[k % 12], Q9f[k - 32]); }
      { const int kk = (k & ~3) + 8 + (3 - (k & 3)); if (kk < 40) fr[kk % 12] = ldf(kk); }
      extra(k);
      SB();
    }
    sl = NEXTSL(sl);
  };
  auto read_scores = [&](int k) {
    if (k < 9) {
#pragma unroll
      for (int i = 0; i < 4; ++i) sv[4 * k + i] = AREAD(192 + 4 * k + i);
    }
    if (k >= 1 && k < 9) {
      const int j = 4 * (k - 1);
      const float v = fmaxf(fmaxf(sv[j], sv[j + 1]), fmaxf(sv[j + 2], sv[j + 3]));
      mt = k == 1 ? v : fmaxf(mt, v);
    }
    if (k == 9) {
      mt9 = fmaxf(fmaxf(sv[32], sv[33]), fmaxf(sv[34], sv[35]));
      mt = fmaxf(mt, __shfl_xor(mt, 32));
      mt9 = fmaxf(mt9, __shfl_xor(mt9, 16));
    }
    if (k == 14) mt9 = fmaxf(mt9, __shfl_xor(mt9, 32));
  };

#pragma unroll
  for (int i = 0; i < 36; ++i) sv[i] = -3.0e38f;
  mt = -3.0e38f; mt9 = -3.0e38f; m = 0.f; m9 = 0.f;
#pragma unroll 1
  for (int t = -1; t < 8; ++t) {
    STAMP(17 + 2 * (t));
    if (t < 0) WAITV(16); else WAITV(24);
    BARRIER();
    STAMP(18 + 2 * (t));
    if (t == 0) { m = mt; m9 = mt9; }
    else if (__any((mt - m > THR) || (mt9 - m9 > THR))) {
      const float mn = fmaxf(m, mt), mn9 = fmaxf(m9, mt9);
      const float al = __builtin_amdgcn_exp2f((m - mn) * CS), al9 = __builtin_amdgcn_exp2f((m9 - mn9) * CS);
      m = mn; m9 = mn9; l *= al; l9 *= al9;
#pragma unroll
      for (int i = 0; i < 128; ++i) ASCALE(i, al);
#pragma unroll
      for (int i = 128; i < 192; ++i) ASCALE(i, al9);
    }
    float mc = m * CS, mc9 = m9 * CS;
    unsigned pk[18];
    float xa[2][2], ea[2][2], lp0 = 0.f, lp1 = 0.f;
    auto sm1 = [&](int pr) {
      const float c_ = pr < 16 ? mc : mc9;
      float s0 = sv[2 * pr], s1 = sv[2 * pr + 1];
      asm volatile("" : "+v"(s0), "+v"(s1));
      xa[pr & 1][0] = fmaf(s0, CS, -c_); xa[pr & 1][1] = fmaf(s1, CS, -c_);
    };
    auto sm2 = [&](int pr) {
      asm volatile("v_exp_f32 %0, %2\n\tv_exp_f32 %1, %3" : "=&v"(ea[pr & 1][0]), "=&v"(ea[pr & 1][1]) : "v"(xa[pr & 1][0]), "v"(xa[pr & 1][1]));
    };
    auto sm3 = [&](int pr) {
      const float e0 = ea[pr & 1][0], e1 = ea[pr & 1][1];
      asm volatile("v_cvt_pk_bf16_f32 %0, %1, %2" : "=v"(pk[pr]) : "v"(e0), "v"(e1));
      if (pr < 16) { asm volatile("v_add_f32 %0, %0, %2\n\tv_add_f32 %1, %1, %3" : "+v"(lp0), "+v"(lp1) : "v"(e0), "v"(e1)); }
      else l9 += e0 + e1;
    };
    {
      if (t == 5) {
#pragma unroll
        for (int n = 0; n < 8; ++n) voffK[n] = 0;
      }
      const int ps = t < 0 ? 1 : PREVSL(sl);
      const char* gk = kg + (size_t)(t < 5 ? t + 3 : 0) * SLOT;
      s_phase([&](int k) {
        if ((k & 1) == 0 && (k >> 1) < 18) sm1(k >> 1);
        if ((k & 1) == 1 && (k >> 1) < 18) sm2(k >> 1);
        if ((k & 1) == 0 && k >= 2 && (k >> 1) - 1 < 18) sm3((k >> 1) - 1);
        if ((k & 3) == 1 && (k >> 2) < 8) pieceKV(gk, true, ps * SLOT, k >> 2);
      });
      l += lp0 + lp1;
    }
#pragma unroll
    for (int i = 0; i < 4; ++i) {
      u32x4 wv = {pk[4 * i], pk[4 * i + 1], pk[4 * i + 2], pk[4 * i + 3]};
      pf[i] = __builtin_bit_cast(bf16x8, wv);
    }
    {
      u32x2 wv = {pk[16], pk[17]};
      p9f = __builtin_bit_cast(bf16x4, wv);
    }
    if (t >= 0) {
      STAMP(35 + 2 * (t));
      WAITV(24);
      BARRIER();
      STAMP(36 + 2 * (t));
      bf16x8 fr[12];
      unsigned bV0 = lds_base + sl * SLOT + laneV; OPAQUE(bV0);
      const unsigned bV1 = (bV0 ^ 32) + 4096;
      unsigned bV9 = lds_base + sl * SLOT + laneV9; OPAQUE(bV9);
      auto ldf = [&](int k) -> bf16x8 {
        if (k < 32) {
          const int kp = k >> 3, d = k & 7;
          return trA2((bV0 ^ ((d & 3) << 6)) + (d >> 2) * 256 + kp * 8192, (bV1 ^ ((d & 3) << 6)) + (d >> 2) * 256 + kp * 8192);
        } else {
          const int d = k - 32;
          return trA2((bV9 ^ ((d & 3) << 6)) + (d >> 2) * 256, ((bV9 ^ 32) ^ ((d & 3) << 6)) + (d >> 2) * 256);
        }
      };
#pragma unroll
      for (int k = 0; k < 8; ++k) { const int kk = (k & ~3) + (3 - (k & 3)); fr[kk] = ldf(kk); }
      if (t == 6) {
#pragma unroll
        for (int n = 0; n < 8; ++n) voffV[n] = 0;
      }
      const int ps = PREVSL(sl);
      const char* gv = vg + (size_t)(t < 6 ? t + 2 : 0) * SLOT;
      SB();
#pragma unroll
      for (int k = 0; k < 40; ++k) {
        if (k < 32) M32(16 * (k & 7), fr[k % 12], pf[k >> 3]);
        else {
          const int d = k - 32;
          bf16x4 lo = __builtin_shufflevector(fr[k % 12], fr[k % 12], 0, 1, 2, 3), hi4 = __builtin_shufflevector(fr[k % 12], fr[k % 12], 4, 5, 6, 7);
          M16x16(128 + 8 * d, lo, p9f);
          M16x16(132 + 8 * d, hi4, p9f);
        }
        { const int kk = (k & ~3) + 8 + (3 - (k & 3)); if (kk < 40) fr[kk % 12] = ldf(kk); }
        if ((k & 3) == 1 && (k >> 2) < 8) pieceKV(gv, false, ps * SLOT, k >> 2);
        if (k >= 2 && k < 17) read_scores(k - 2);
        SB();
      }
      sl = NEXTSL(sl);
    } else {
      BARRIER();
#pragma unroll
      for (int n = 0; n < 8; ++n) pieceKV(vg + SLOT, false, 0, n);
      float dm = 0.f; (void)dm;
#pragma unroll
      for (int k = 0; k < 15; ++k) read_scores(k);
    }
  }
  WAITV(0);
  NOPS();
  l += __shfl_xor(l, 32);
  l9 += __shfl_xor(l9, 16);
  l9 += __shfl_xor(l9, 32);
  const float inv_l = 1.0f / l;

  STAMP(52);
  char* ofp = (char*)Of + (size_t)p * OF_PER_P * 2;
  {
    char* dst = ofp + ((size_t)(4 * b + w) * 256 + h * 16) * 1024 + lane * 16;
#pragma unroll
    for (int s = 0; s < 16; ++s) {
      const int A = 16 * (s >> 1) + 8 * (s & 1);
      bf16x8 v = pack8(AREAD(A) * inv_l, AREAD(A + 1) * inv_l, AREAD(A + 2) * inv_l, AREAD(A + 3) * inv_l,
                       AREAD(A + 4) * inv_l, AREAD(A + 5) * inv_l, AREAD(A + 6) * inv_l, AREAD(A + 7) * inv_l);
      *reinterpret_cast<bf16x8*>(dst + s * 1024) = v;
    }
  }
  RELANE();
  BARRIER();
  {
    float* st = reinterpret_cast<float*>(smem + 4 * 16 * 1040);
#pragma unroll
    for (int d = 0; d < 16; ++d) {
      f32x4 v = {AREAD(128 + 4 * d), AREAD(129 + 4 * d), AREAD(130 + 4 * d), AREAD(131 + 4 * d)};
      *reinterpret_cast<f32x4*>(smem + (w * 16 + i16) * 1040 + (16 * d + 4 * g) * 4) = v;
    }
    if (g == 0) { st[(w * 16 + i16) * 2] = m9 * CS; st[(w * 16 + i16) * 2 + 1] = l9; }
    BARRIER();
#pragma unroll
    for (int it = 0; it < 2; ++it) {
      const int item = tid + 256 * it;
      const int q = item >> 5, ck = item & 31;
      float mw[4], lw[4];
#pragma unroll
      for (int ww = 0; ww < 4; ++ww) { mw[ww] = st[(ww * 16 + q) * 2]; lw[ww] = st[(ww * 16 + q) * 2 + 1]; }
      const float M = fmaxf(fmaxf(mw[0], mw[1]), fmaxf(mw[2], mw[3]));
      float f[4], L = 0.f;
#pragma unroll
      for (int ww = 0; ww < 4; ++ww) { f[ww] = __builtin_amdgcn_exp2f(mw[ww] - M); L += f[ww] * lw[ww]; }
      const float il = 1.0f / L;
      float o[8];
#pragma unroll
      for (int j = 0; j < 8; ++j) o[j] = 0.f;
#pragma unroll
      for (int ww = 0; ww < 4; ++ww) {
        const f32x4 a = *reinterpret_cast<const f32x4*>(smem + (ww * 16 + q) * 1040 + ck * 32);
        const f32x4 c2 = *reinterpret_cast<const f32x4*>(smem + (ww * 16 + q) * 1040 + ck * 32 + 16);
#pragma unroll
        for (int j = 0; j < 4; ++j) { o[j] += f[ww] * a[j]; o[4 + j] += f[ww] * c2[j]; }
      }
      bf16x8 v = pack8(o[0] * il, o[1] * il, o[2] * il, o[3] * il, o[4] * il, o[5] * il, o[6] * il, o[7] * il);
      char* dst = ofp + ((size_t)(32 + (b >> 1)) * 256 + h * 16 + (ck >> 1)) * 1024 + (16 * (b & 1) + q + 32 * (ck & 1)) * 16;
      *reinterpret_cast<bf16x8*>(dst) = v;
    }
  }
  STAMP(53);
}

constexpr int K2_NBUF = 5;
constexpr int K2_WAVE_LDS = K2_NBUF * 4096;
DI bf16x8 gload16_asm(const char* p) {
  bf16x8 r;
  asm volatile("global_load_dwordx4 %0, %1, off" : "=v"(r) : "v"(p) : "memory");
  return r;
}
__global__ __launch_bounds__(512, 2) void oproj_kernel(const __hip_bfloat16* __restrict__ Of, const __hip_bfloat16* __restrict__ woA,
                                                       const __hip_bfloat16* __restrict__ woB, __hip_bfloat16* __restrict__ out) {
  extern __shared__ __attribute__((aligned(16))) char smem[];
  const int tid = threadIdx.x, lane = tid & 63;
  const int w = __builtin_amdgcn_readfirstlane(tid >> 6);
  const int T = (blockIdx.x & 7) * 32 + (blockIdx.x >> 3);
  if (T >= 252) return;
  const int p = T / 126, rem = T % 126, mg = rem / 7, ng = rem % 7;
  const char* of = (const char*)Of + (size_t)p * OF_PER_P * 2 + ((size_t)(2 * mg) * 256 + w * 32) * 1024 + lane * 16;
  const char* wo = (const char*)(p ? woB : woA) + (size_t)(w * 512) * (DM * 2) + ng * 128;
  const int hi = lane >> 5, blk = (lane >> 4) & 1, q4 = (lane & 15) >> 2, pp = lane & 3;
  const unsigned lds_base = (unsigned)(size_t)(lds_b4_ptr)smem + w * K2_WAVE_LDS;
  const unsigned voff = (unsigned)((lane >> 3) * (DM * 2) + (((lane & 7) ^ (((lane >> 4) & 1) << 2)) << 4));

  bf16x8 ob[K2_NBUF][2][2];
  auto issue = [&](int ck) {
#pragma unroll
    for (int mt = 0; mt < 2; ++mt)
#pragma unroll
      for (int ks = 0; ks < 2; ++ks) ob[ck % K2_NBUF][mt][ks] = gload16_asm(of + ((size_t)mt * 256 + 2 * ck + ks) * 1024);
#pragma unroll
    for (int n = 0; n < 4; ++n)
      glds16(wo + (size_t)(32 * ck + 8 * n) * (DM * 2), voff, lds_base + (ck % K2_NBUF) * 4096 + n * 1024);
  };
  f32x16 acc[2][2];
#pragma unroll
  for (int a = 0; a < 2; ++a)
#pragma unroll
    for (int c = 0; c < 2; ++c) acc[a][c] = f32x16{};
  issue(0); issue(1); issue(2); issue(3);
#pragma unroll
  for (int ck = 0; ck < 16; ++ck) {
    if (ck + 4 < 16) { issue(ck + 4); WAITV(32); }
    else if (ck == 12) WAITV(24); else if (ck == 13) WAITV(16); else if (ck == 14) WAITV(8); else WAITV(0);
    {
      bf16x8 (&o)[2][2] = ob[ck % K2_NBUF];
      asm volatile("" : "+v"(o[0][0]), "+v"(o[0][1]), "+v"(o[1][0]), "+v"(o[1][1]));
    }
    const unsigned sb = w * K2_WAVE_LDS + (ck % K2_NBUF) * 4096;
    bf16x8 fa[2][2];
#pragma unroll
    for (int ks = 0; ks < 2; ++ks)
#pragma unroll
      for (int nt = 0; nt < 2; ++nt) {
        const int row0 = 16 * ks + 8 * hi + q4, row1 = row0 + 4;
        const int ch = 4 * nt + 2 * blk + (pp >> 1);
        fa[ks][nt] = tr_read2(smem, sb + row0 * 128 + ((ch ^ (((row0 >> 1) & 1) << 2)) << 4) + 8 * (pp & 1),
                              sb + row1 * 128 + ((ch ^ (((row1 >> 1) & 1) << 2)) << 4) + 8 * (pp & 1));
      }
#pragma unroll
    for (int ks = 0; ks < 2; ++ks)
#pragma unroll
      for (int nt = 0; nt < 2; ++nt)
#pragma unroll
        for (int mt = 0; mt < 2; ++mt) acc[nt][mt] = mfma32(fa[ks][nt], ob[ck % K2_NBUF][mt][ks], acc[nt][mt]);
    asm volatile("s_waitcnt lgkmcnt(0)" ::: "memory");
  }
  __syncthreads();
  const int r32 = lane & 31;
#pragma unroll
  for (int nt = 0; nt < 2; ++nt)
#pragma unroll
    for (int mt = 0; mt < 2; ++mt)
#pragma unroll
      for (int qd = 0; qd < 4; ++qd) {
        f32x4 v = {acc[nt][mt][4 * qd], acc[nt][mt][4 * qd + 1], acc[nt][mt][4 * qd + 2], acc[nt][mt][4 * qd + 3]};
        *reinterpret_cast<f32x4*>(smem + ((size_t)w * 64 + 32 * mt + r32) * 272 + (32 * nt + 8 * qd + 4 * hi) * 4) = v;
      }
  __syncthreads();
  {
    const int m = tid >> 3, nc = tid & 7;
    float o[8];
#pragma unroll
    for (int j = 0; j < 8; ++j) o[j] = 0.f;
#pragma unroll
    for (int ww = 0; ww < 8; ++ww) {
      const f32x4 a = *reinterpret_cast<const f32x4*>(smem + ((size_t)ww * 64 + m) * 272 + nc * 32);
      const f32x4 c = *reinterpret_cast<const f32x4*>(smem + ((size_t)ww * 64 + m) * 272 + nc * 32 + 16);
#pragma unroll
      for (int j = 0; j < 4; ++j) { o[j] += a[j]; o[4 + j] += c[j]; }
    }
    const int mt = 2 * mg + (m >> 5), r = m & 31;
    int row;
    if (mt < 32) row = (mt >> 2) * SQ + (mt & 3) * 32 + r;
    else row = (2 * (mt - 32) + (r >> 4)) * SQ + 128 + (r & 15);
    bf16x8 v = pack8(o[0], o[1], o[2], o[3], o[4], o[5], o[6], o[7]);
    *reinterpret_cast<bf16x8*>((char*)out + ((size_t)row * (2 * DM) + p * DM + ng * 64 + nc * 8) * 2) = v;
  }
}

extern "C" void kernel_launch(void* const* d_in, const int* in_sizes, int n_in, void* d_out, int out_size, void* d_ws,
                              size_t ws_size, hipStream_t stream) {
  const __hip_bfloat16* xA = (const __hip_bfloat16*)d_in[0];
  const __hip_bfloat16* kA = (const __hip_bfloat16*)d_in[1];
  const __hip_bfloat16* vA = (const __hip_bfloat16*)d_in[2];
  const __hip_bfloat16* wqA = (const __hip_bfloat16*)d_in[3];
  const __hip_bfloat16* woA = (const __hip_bfloat16*)d_in[4];
  const __hip_bfloat16* xB = (const __hip_bfloat16*)d_in[5];
  const __hip_bfloat16* kB = (const __hip_bfloat16*)d_in[6];
  const __hip_bfloat16* vB = (const __hip_bfloat16*)d_in[7];
  const __hip_bfloat16* wqB = (const __hip_bfloat16*)d_in[8];
  const __hip_bfloat16* woB = (const __hip_bfloat16*)d_in[9];
  __hip_bfloat16* Of = (__hip_bfloat16*)d_ws;
  hipFuncSetAttribute((const void*)attn_kernel, hipFuncAttributeMaxDynamicSharedMemorySize, LDS_BYTES);
  hipFuncSetAttribute((const void*)oproj_kernel, hipFuncAttributeMaxDynamicSharedMemorySize, 8 * K2_WAVE_LDS);
  attn_kernel<<<256, 256, LDS_BYTES, stream>>>(xA, kA, vA, wqA, xB, kB, vB, wqB, Of, woA, woB
  );
  oproj_kernel<<<256, 512, 8 * K2_WAVE_LDS, stream>>>(Of, woA, woB, (__hip_bfloat16*)d_out);
}
```

```cpp
#include <hip/hip_runtime.h>
#include <hip/hip_bf16.h>
#include <stdint.h>
#include <type_traits>
#define ILQ 2
#define ILF 4

typedef short bf16x8 __attribute__((ext_vector_type(8)));
typedef short bf16x4 __attribute__((ext_vector_type(4)));
typedef float f32x16 __attribute__((ext_vector_type(16)));
typedef float f32x4 __attribute__((ext_vector_type(4)));
typedef unsigned u32x4 __attribute__((ext_vector_type(4)));
typedef unsigned u32x2 __attribute__((ext_vector_type(2)));
typedef __bf16 bf2_t __attribute__((ext_vector_type(2)));
typedef __attribute__((address_space(3))) bf16x4* lds_b4_ptr;

#define DI __device__ __forceinline__

constexpr int NB = 8, SQ = 144, DM = 448, NH = 16, SK = 512, DH = 256;
constexpr int SLOT = 32768;
constexpr int LDS_BYTES = 5 * SLOT;
constexpr size_t OF_PER_P = (size_t)36 * 256 * 512;

DI unsigned pack2(float a, float b) {
  bf2_t v = {(__bf16)a, (__bf16)b};
  return __builtin_bit_cast(unsigned, v);
}
DI bf16x8 pack8(float a0, float a1, float a2, float a3, float a4, float a5, float a6, float a7) {
  u32x4 w = {pack2(a0, a1), pack2(a2, a3), pack2(a4, a5), pack2(a6, a7)};
  return __builtin_bit_cast(bf16x8, w);
}
DI f32x16 mfma32(bf16x8 a, bf16x8 b, f32x16 c) { return __builtin_amdgcn_mfma_f32_32x32x16_bf16(a, b, c, 0, 0, 0); }
DI f32x4 mfma16x32(bf16x8 a, bf16x8 b, f32x4 c) { return __builtin_amdgcn_mfma_f32_16x16x32_bf16(a, b, c, 0, 0, 0); }
DI f32x4 mfma16x16(bf16x4 a, bf16x4 b, f32x4 c) { return __builtin_amdgcn_mfma_f32_16x16x16bf16_1k(a, b, c, 0, 0, 0); }

#define WAITV(n) asm volatile("s_waitcnt vmcnt(%0)" ::"n"(n) : "memory")
#define SB() __builtin_amdgcn_sched_barrier(0)
#define STAMP(i)
#define BARRIER() do { asm volatile("s_waitcnt lgkmcnt(0)" ::: "memory"); __builtin_amdgcn_s_barrier(); asm volatile("" ::: "memory"); } while (0)

DI bf16x4 tr_read(const char* smem, unsigned off) {
  return __builtin_amdgcn_ds_read_tr16_b64_v4i16((lds_b4_ptr)(smem + off));
}
DI bf16x8 tr_read2(const char* smem, unsigned off0, unsigned off1) {
  bf16x4 lo = tr_read(smem, off0), hi = tr_read(smem, off1);
  return __builtin_shufflevector(lo, hi, 0, 1, 2, 3, 4, 5, 6, 7);
}
DI void glds16(const char* gbase, unsigned voff, unsigned ldst) {
  unsigned keep;
  asm volatile("s_mov_b32 %0, m0\n\ts_mov_b32 m0, %3\n\ts_nop 0\n\tglobal_load_lds_dwordx4 %1, %2\n\ts_mov_b32 m0, %0"
               : "=&s"(keep) : "v"(voff), "s"(gbase), "s"(ldst) : "memory");
}
DI void glds16nt(const char* gbase, unsigned voff, unsigned ldst) {
  unsigned keep;
  asm volatile("s_mov_b32 %0, m0\n\ts_mov_b32 m0, %3\n\ts_nop 0\n\tglobal_load_lds_dwordx4 %1, %2 nt\n\ts_mov_b32 m0, %0"
               : "=&s"(keep) : "v"(voff), "s"(gbase), "s"(ldst) : "memory");
}
DI bf16x8 lds128(const char* smem, unsigned off) { return *reinterpret_cast<const bf16x8*>(smem + off); }

typedef __attribute__((address_space(3))) bf16x8* lds_b8_ptr;
DI bf16x4 trA(unsigned addr) { return __builtin_amdgcn_ds_read_tr16_b64_v4i16(reinterpret_cast<lds_b4_ptr>(addr)); }
DI bf16x8 trA2(unsigned a0, unsigned a1) {
  bf16x4 lo = trA(a0), hi = trA(a1);
  return __builtin_shufflevector(lo, hi, 0, 1, 2, 3, 4, 5, 6, 7);
}
DI bf16x8 ldA(unsigned addr) { return *reinterpret_cast<lds_b8_ptr>(addr); }
DI void ascale(float& a, float s) {
  float tmp;
  asm volatile("v_accvgpr_read_b32 %1, %0\n\tv_mul_f32 %1, %1, %2\n\tv_accvgpr_write_b32 %0, %1" : "+a"(a), "=&v"(tmp) : "v"(s));
}
DI void amfma32(f32x16& c, bf16x8 a, bf16x8 b) { asm volatile("v_mfma_f32_32x32x16_bf16 %0, %1, %2, %0" : "+a"(c) : "v"(a), "v"(b)); }
DI void amfma32z(f32x16& c, bf16x8 a, bf16x8 b) { asm volatile("v_mfma_f32_32x32x16_bf16 %0, %1, %2, 0" : "=a"(c) : "v"(a), "v"(b)); }
DI void amfma16x32(f32x4& c, bf16x8 a, bf16x8 b) { asm volatile("v_mfma_f32_16x16x32_bf16 %0, %1, %2, %0" : "+a"(c) : "v"(a), "v"(b)); }
DI void amfma16x32z(f32x4& c, bf16x8 a, bf16x8 b) { asm volatile("v_mfma_f32_16x16x32_bf16 %0, %1, %2, 0" : "=a"(c) : "v"(a), "v"(b)); }
DI void amfma16x16(f32x4& c, bf16x4 a, bf16x4 b) { asm volatile("v_mfma_f32_16x16x16_bf16 %0, %1, %2, %0" : "+a"(c) : "v"(a), "v"(b)); }
#define MFMA_DRAIN() asm volatile("s_nop 15\n\ts_nop 15" ::: "memory")
#define OPAQUE(x) asm volatile("" : "+v"(x))

DI int swzT(int row) { return ((row & 3) << 2) | ((((row >> 2) ^ (row >> 3)) & 1) << 1); }
DI int swap2(int p) { return ((p & 1) << 1) | (p >> 1); }

constexpr int STG = 53248;

#define ALL_AGPRS "a0", "a1", "a2", "a3", "a4", "a5", "a6", "a7", "a8", "a9", "a10", "a11", "a12", "a13", "a14", "a15", "a16", "a17", "a18", "a19", "a20", "a21", "a22", "a23", "a24", "a25", "a26", "a27", "a28", "a29", "a30", "a31", "a32", "a33", "a34", "a35", "a36", "a37", "a38", "a39", "a40", "a41", "a42", "a43", "a44", "a45", "a46", "a47", "a48", "a49", "a50", "a51", "a52", "a53", "a54", "a55", "a56", "a57", "a58", "a59", "a60", "a61", "a62", "a63", "a64", "a65", "a66", "a67", "a68", "a69", "a70", "a71", "a72", "a73", "a74", "a75", "a76", "a77", "a78", "a79", "a80", "a81", "a82", "a83", "a84", "a85", "a86", "a87", "a88", "a89", "a90", "a91", "a92", "a93", "a94", "a95", "a96", "a97", "a98", "a99", "a100", "a101", "a102", "a103", "a104", "a105", "a106", "a107", "a108", "a109", "a110", "a111", "a112", "a113", "a114", "a115", "a116", "a117", "a118", "a119", "a120", "a121", "a122", "a123", "a124", "a125", "a126", "a127", "a128", "a129", "a130", "a131", "a132", "a133", "a134", "a135", "a136", "a137", "a138", "a139", "a140", "a141", "a142", "a143", "a144", "a145", "a146", "a147", "a148", "a149", "a150", "a151", "a152", "a153", "a154", "a155", "a156", "a157", "a158", "a159", "a160", "a161", "a162", "a163", "a164", "a165", "a166", "a167", "a168", "a169", "a170", "a171", "a172", "a173", "a174", "a175", "a176", "a177", "a178", "a179", "a180", "a181", "a182", "a183", "a184", "a185", "a186", "a187", "a188", "a189", "a190", "a191", "a192", "a193", "a194", "a195", "a196", "a197", "a198", "a199", "a200", "a201", "a202", "a203", "a204", "a205", "a206", "a207", "a208", "a209", "a210", "a211", "a212", "a213", "a214", "a215", "a216", "a217", "a218", "a219", "a220", "a221", "a222", "a223", "a224", "a225", "a226", "a227", "a228", "a229", "a230", "a231", "a232", "a233", "a234", "a235", "a236", "a237", "a238", "a239", "a240", "a241", "a242", "a243", "a244", "a245", "a246", "a247", "a248", "a249", "a250", "a251", "a252", "a253", "a254", "a255"
DI void M32(int A, bf16x8 a, bf16x8 b) { asm volatile("v_mfma_f32_32x32x16_bf16 a[%c2:%c3], %0, %1, a[%c2:%c3]" ::"v"(a), "v"(b), "i"(A), "i"(A + 15) : ALL_AGPRS); }
DI void M32Z(int A, bf16x8 a, bf16x8 b) { asm volatile("v_mfma_f32_32x32x16_bf16 a[%c2:%c3], %0, %1, 0" ::"v"(a), "v"(b), "i"(A), "i"(A + 15) : ALL_AGPRS); }
DI void M16x32(int A, bf16x8 a, bf16x8 b) { asm volatile("v_mfma_f32_16x16x32_bf16 a[%c2:%c3], %0, %1, a[%c2:%c3]" ::"v"(a), "v"(b), "i"(A), "i"(A + 3) : ALL_AGPRS); }
DI void M16x32Z(int A, bf16x8 a, bf16x8 b) { asm volatile("v_mfma_f32_16x16x32_bf16 a[%c2:%c3], %0, %1, 0" ::"v"(a), "v"(b), "i"(A), "i"(A + 3) : ALL_AGPRS); }
DI void M16x16(int A, bf16x4 a, bf16x4 b) { asm volatile("v_mfma_f32_16x16x16_bf16 a[%c2:%c3], %0, %1, a[%c2:%c3]" ::"v"(a), "v"(b), "i"(A), "i"(A + 3) : ALL_AGPRS); }
DI float AREAD(int A) { float r; asm volatile("v_accvgpr_read_b32 %0, a%c1" : "=v"(r) : "i"(A)); return r; }
DI void AZERO(int A) { asm volatile("v_accvgpr_write_b32 a%c0, 0" ::"i"(A) : ALL_AGPRS); }
DI void ASCALE(int A, float s) {
  float t;
  asm volatile("v_accvgpr_read_b32 %0, a%c2\n\tv_mul_f32 %0, %0, %1\n\tv_accvgpr_write_b32 a%c2, %0" : "=&v"(t) : "v"(s), "i"(A));
}
#define NOPS() asm volatile("s_nop 15\n\ts_nop 15")
template <int OFF, bool NT>
DI void dma(const char* gbase, unsigned voff, unsigned ldst) {
  if (NT) asm volatile("s_mov_b32 m0, %2\n\ts_nop 0\n\tglobal_load_lds_dwordx4 %0, %1 offset:%c3 nt" ::"v"(voff), "s"(gbase), "s"(ldst), "i"(OFF) : "memory");
  else asm volatile("s_mov_b32 m0, %2\n\ts_nop 0\n\tglobal_load_lds_dwordx4 %0, %1 offset:%c3" ::"v"(voff), "s"(gbase), "s"(ldst), "i"(OFF) : "memory");
}

template <bool NT>
DI void dmap(const char* gbase, unsigned voff, unsigned ldst, int flag) {
  asm volatile("s_cmp_eq_u32 %3, 0\n\ts_cbranch_scc1 1f\n\ts_mov_b32 m0, %2\n\ts_nop 0\n\tglobal_load_lds_dwordx4 %0, %1\n1:"
               ::"v"(voff), "s"(gbase), "s"(ldst), "s"(flag) : "memory", "scc");
}
DI void zero_acc() {
  bf16x8 z = {};
  asm volatile("" : "+v"(z));
#pragma unroll
  for (int i = 0; i < 12; ++i) M32Z(16 * i, z, z);
}

__global__ __launch_bounds__(256, 1) void attn_kernel(
    const __hip_bfloat16* __restrict__ xA, const __hip_bfloat16* __restrict__ kA, const __hip_bfloat16* __restrict__ vA,
    const __hip_bfloat16* __restrict__ wqA, const __hip_bfloat16* __restrict__ xB, const __hip_bfloat16* __restrict__ kB,
    const __hip_bfloat16* __restrict__ vB, const __hip_bfloat16* __restrict__ wqB, __hip_bfloat16* __restrict__ Of,
    const __hip_bfloat16* __restrict__ woA, const __hip_bfloat16* __restrict__ woB
    ) {
  extern __shared__ __attribute__((aligned(16))) char smem[];
  asm volatile("" ::: "a0", "a64", "a128", "a192", "a227");
  const int tid = threadIdx.x, lane = tid & 63;
  const int w = __builtin_amdgcn_readfirstlane(tid >> 6);
  const int bid = blockIdx.x, xcd = bid & 7, idx = bid >> 3;
  const int ph = xcd * 4 + (idx >> 3), p = ph >> 4, h = ph & 15, b = idx & 7;
  const char* xg = (const char*)(p ? xB : xA) + (size_t)b * SQ * DM * 2;
  const char* wg = (const char*)(p ? wqB : wqA) + (size_t)h * DH * 2;
  const char* kg = (const char*)(p ? kB : kA) + (size_t)(b * NH + h) * SK * DH * 2;
  const char* vg = (const char*)(p ? vB : vA) + (size_t)(b * NH + h) * SK * DH * 2;

  int r32 = lane & 31, hi = lane >> 5, i16 = lane & 15, g = lane >> 4;
  int blk = (lane >> 4) & 1, q4 = (lane & 15) >> 2, pp = lane & 3;
#define RELANE() do { int l_ = lane; asm volatile("" : "+v"(l_)); r32 = l_ & 31; hi = l_ >> 5; i16 = l_ & 15; g = l_ >> 4; \
    blk = (l_ >> 4) & 1; q4 = (l_ & 15) >> 2; pp = l_ & 3; } while (0)

  const unsigned lds_base = (unsigned)(size_t)(lds_b4_ptr)smem;
  unsigned voffK[8], voffV[8], voffW[8], voffX[5];
#pragma unroll
  for (int n = 0; n < 8; ++n) {
    const unsigned xk = (unsigned)(2 * n), xv = (unsigned)((((2 * n) & 3) << 2) | ((((n >> 1) ^ (n >> 2)) & 1) << 1));
    voffK[n] = (unsigned)((16 * w + hi) * 512 + (((r32 ^ hi) ^ xk) << 4));
    voffV[n] = (unsigned)((16 * w + hi) * 512 + (((r32 ^ (hi << 2)) ^ xv) << 4));
    voffW[n] = (unsigned)((16 * w + hi + 2 * n) * 8192 + (((r32 ^ (hi << 2)) ^ xv) << 4));
  }
#pragma unroll
  for (int n = 0; n < 5; ++n) {
    const int gi = 5 * w + n;
    int row = 8 * gi + (lane >> 3);
    const int ch = (lane & 7) ^ ((row >> 1) & 7);
    row = row < SQ ? row : SQ - 1;
    voffX[n] = (unsigned)(row * (DM * 2) + ch * 16);
  }
  const unsigned ldsw = lds_base + w * 8192;
  auto pieceKV = [&](const char* gb, bool isK, unsigned ldst, int n) {
    const unsigned vo = isK ? voffK[n] : voffV[n];
    const char* g2 = gb + (n >> 2) * 4096;
    const unsigned l2 = ldsw + ldst + (n >> 2) * 4096;
    switch (n & 3) {
      case 0: dma<0, true>(g2, vo, l2); break;
      case 1: dma<1024, true>(g2, vo, l2); break;
      case 2: dma<2048, true>(g2, vo, l2); break;
      default: dma<3072, true>(g2, vo, l2); break;
    }
  };
  auto pieceQ = [&](const char* wgc, const char* xgc, unsigned ldst, int n) {
    if (n < 8) dma<0, false>(wgc, voffW[n], ldsw + ldst + n * 1024);
    else dma<0, false>(xgc, voffX[n - 8], lds_base + ldst + SLOT + (5 * w + n - 8) * 1024);
  };
  STAMP(0);
#pragma unroll
  for (int n = 0; n < 13; ++n) pieceQ(wg, xg, 0, n);
#pragma unroll
  for (int n = 0; n < 13; ++n) pieceQ(wg + 64 * NH * DH * 2, xg + 128, STG, n);
  {
    const char* wop = (const char*)(p ? woB : woA) + (size_t)(b * NH + h) * 28672 + w * 7168;
#pragma unroll
    for (int n = 0; n < 7; ++n) dma<0, false>(wop + n * 1024, (unsigned)(lane * 16), lds_base + 3 * STG + w * 1024);
  }
  zero_acc();

  {
    const int spw = swap2(pp);
    const unsigned laneW = (unsigned)((8 * hi + q4) * 512 + ((((q4 << 2) | ((blk ^ hi) << 1)) ^ (spw >> 1)) << 4) + 8 * (spw & 1));
    const unsigned lane9 = (unsigned)((8 * g + q4) * 512 + (((8 * w + (pp >> 1)) ^ ((q4 << 2) | ((g & 1) << 1))) << 4) + 8 * (pp & 1));
    const unsigned laneX = (unsigned)(SLOT + (32 * w + r32) * 128 + ((hi ^ ((r32 >> 1) & 7)) << 4));
    const unsigned laneX9 = (unsigned)(SLOT + (128 + i16) * 128 + ((g ^ ((i16 >> 1) & 7)) << 4));
    unsigned stg = 0;
#pragma unroll 1
    for (int c = 0; c < 7; ++c) {
      STAMP(1 + 2 * c);
      if (c < 2) WAITV(20); else if (c < 6) WAITV(13); else WAITV(8);
      BARRIER();
      STAMP(2 + 2 * c);
      bf16x8 fr[12], xq[4], x9q[2];
      unsigned bW0 = lds_base + stg + laneW; OPAQUE(bW0);
      unsigned bX = lds_base + stg + laneX; OPAQUE(bX);
      unsigned bX9 = lds_base + stg + laneX9; OPAQUE(bX9);
      const unsigned bW1 = (bW0 ^ 32) + 2048;
      unsigned b9 = lds_base + stg + lane9; OPAQUE(b9);
      auto ldf = [&](int k) -> bf16x8 {
        if (k < 32) {
          const int ks = k >> 3, d = k & 7;
          return trA2((bW0 ^ ((d & 3) << 6)) + (d >> 2) * 256 + ks * 8192, (bW1 ^ ((d & 3) << 6)) + (d >> 2) * 256 + ks * 8192);
        } else {
          const int ks = (k - 32) >> 2, d = (k - 32) & 3;
          return trA2((b9 ^ (d << 5)) + ks * 16384, ((b9 ^ 32) ^ (d << 5)) + 2048 + ks * 16384);
        }
      };
#pragma unroll
      for (int ks = 0; ks < 4; ++ks) xq[ks] = ldA(bX ^ (ks << 5));
#pragma unroll
      for (int ks = 0; ks < 2; ++ks) x9q[ks] = ldA(bX9 ^ (ks << 6));
#pragma unroll
      for (int k = 0; k < 8; ++k) { const int kk = (k & ~3) + (3 - (k & 3)); fr[kk] = ldf(kk); }
      unsigned nx = stg + 2 * STG; nx = nx >= 3 * STG ? nx - 3 * STG : nx;
      const char* wgc = wg + (size_t)(c + 2) * (64 * NH * DH * 2);
      const char* xgc = xg + (c + 2) * 128;
      if (c == 5) {
#pragma unroll
        for (int n = 0; n < 8; ++n) pieceKV(kg, true, 2 * SLOT, n);
      } else if (c == 6) {
#pragma unroll
        for (int n = 0; n < 8; ++n) pieceKV(kg + SLOT, true, 3 * SLOT, n);
#pragma unroll
        for (int n = 0; n < 8; ++n) pieceKV(vg, false, 4 * SLOT, n);
      }
      const int fq = __builtin_amdgcn_readfirstlane(c <= 4);
      SB();
#pragma unroll
      for (int k = 0; k < 40; ++k) {
        if (k < 32) M32(16 * (k & 7), fr[k % 12], xq[k >> 3]);
        else M16x32(128 + 4 * ((k - 32) & 3), fr[k % 12], x9q[(k - 32) >> 2]);
        { const int kk = (k & ~3) + 8 + (3 - (k & 3)); if (kk < 40) fr[kk % 12] = ldf(kk); }
        if ((k % 3) == 0 && k / 3 < 13) {
          const int n = k / 3;
          if (n < 8) dmap<false>(wgc, voffW[n], ldsw + nx + n * 1024, fq);
          else dmap<false>(xgc, voffX[n - 8], lds_base + nx + SLOT + (5 * w + n - 8) * 1024, fq);
        }
        SB();
      }
      stg += STG; stg = stg >= 3 * STG ? 0 : stg;
    }
  }
  NOPS();
  bf16x8 Qf[16];
#pragma unroll
  for (int s = 0; s < 16; ++s) {
    const int A = 16 * (s >> 1) + 8 * (s & 1);
    Qf[s] = pack8(AREAD(A), AREAD(A + 1), AREAD(A + 2), AREAD(A + 3), AREAD(A + 4), AREAD(A + 5), AREAD(A + 6), AREAD(A + 7));
  }
  RELANE();
  BARRIER();
#pragma unroll
  for (int d = 0; d < 4; ++d) {
    u32x2 v = {pack2(AREAD(128 + 4 * d), AREAD(129 + 4 * d)), pack2(AREAD(130 + 4 * d), AREAD(131 + 4 * d))};
    const int dh = 64 * w + 16 * d + 4 * g;
    *reinterpret_cast<u32x2*>(smem + SLOT + i16 * 512 + (((dh >> 3) ^ i16) << 4) + ((dh & 4) << 1)) = v;
  }
  zero_acc();
  bf16x8 Q9f[8];
  BARRIER();
#pragma unroll
  for (int s = 0; s < 8; ++s) Q9f[s] = ldA(lds_base + SLOT + i16 * 512 + (((4 * s + g) ^ i16) << 4));

  constexpr float CS = 0.0625f * 1.4426950408889634f;
  constexpr float THR = 8.0f / CS;
  float m = -3.0e38f, l = 0.f, m9 = -3.0e38f, l9 = 0.f;
  const unsigned laneK = (unsigned)(r32 * 512 + ((hi ^ (r32 & 15)) << 4));
  const unsigned laneK9 = (unsigned)((16 * w + i16) * 512 + ((g ^ i16) << 4));
  const int spv = swap2(pp);
  const unsigned laneV = (unsigned)((4 * hi + q4) * 512 + ((((q4 << 2) | ((blk ^ hi) << 1)) ^ (spv >> 1)) << 4) + 8 * (spv & 1));
  const unsigned laneV9 = (unsigned)((16 * w + 4 * g + q4) * 512 + (((pp >> 1) ^ ((q4 << 2) | (((g ^ (g >> 1)) & 1) << 1))) << 4) + 8 * (pp & 1));
#define NEXTSL(x) ((x) == 4 ? 1 : (x) == 1 ? 0 : (x) == 0 ? 2 : (x) + 1)
#define PREVSL(x) ((x) == 1 ? 4 : (x) == 0 ? 1 : (x) == 2 ? 0 : (x) - 1)
  int sl = 2;
  float sv[36];
  float mt, mt9;
  bf16x8 pf[4];
  bf16x4 p9f;

  auto s_phase = [&](auto&& extra) {
    bf16x8 fr[12];
    unsigned bK = lds_base + sl * SLOT + laneK; OPAQUE(bK);
    unsigned bK9 = lds_base + sl * SLOT + laneK9; OPAQUE(bK9);
    auto ldf = [&](int k) -> bf16x8 {
      if (k < 32) return ldA((bK ^ ((k & 7) << 5)) + ((k >> 3) & 1) * 256 + (k >> 4) * 16384);
      else return ldA((bK9 ^ ((k & 3) << 6)) + ((k >> 2) & 1) * 256);
    };
#pragma unroll
    for (int k = 0; k < 8; ++k) { const int kk = (k & ~3) + (3 - (k & 3)); fr[kk] = ldf(kk); }
    SB();
#pragma unroll
    for (int k = 0; k < 40; ++k) {
      if (k < 32) { if ((k & 15) == 0) M32Z(192 + 16 * (k >> 4), fr[k % 12], Qf[k & 15]); else M32(192 + 16 * (k >> 4), fr[k % 12], Qf[k & 15]); }
      else { if (k == 32) M16x32Z(224, fr[k % 12], Q9f[k - 32]); else M16x32(224, fr[k % 12], Q9f[k - 32]); }
      { const int kk = (k & ~3) + 8 + (3 - (k & 3)); if (kk < 40) fr[kk % 12] = ldf(kk); }
      extra(k);
      SB();
    }
    sl = NEXTSL(sl);
  };
  float mtb;
  auto read_scores = [&](int k) {
    if (k < 9) {
#pragma unroll
      for (int i = 0; i < 4; ++i) sv[4 * k + i] = AREAD(192 + 4 * k + i);
    }
    if (k == 1) {
      asm volatile("v_max_f32 %0, %2, %3\n\tv_max_f32 %1, %4, %5" : "=&v"(mt), "=&v"(mtb) : "v"(sv[0]), "v"(sv[1]), "v"(sv[2]), "v"(sv[3]));
    } else if (k >= 2 && k < 9) {
      const int j = 4 * (k - 1);
      asm volatile("v_max3_f32 %0, %0, %2, %3\n\tv_max3_f32 %1, %1, %4, %5" : "+v"(mt), "+v"(mtb) : "v"(sv[j]), "v"(sv[j + 1]), "v"(sv[j + 2]), "v"(sv[j + 3]));
    }
    if (k == 9) {
      asm volatile("v_max_f32 %0, %0, %2\n\tv_max3_f32 %1, %3, %4, %5" : "+v"(mt), "=&v"(mt9) : "v"(mtb), "v"(sv[32]), "v"(sv[33]), "v"(sv[34]));
    }
    if (k == 10) {
      asm volatile("v_max_f32 %0, %0, %1" : "+v"(mt9) : "v"(sv[35]));
      mtb = __shfl_xor(mt, 32);
    }
    if (k == 11) mtb = fmaxf(mt, mtb), mt = mtb, mtb = __shfl_xor(mt9, 16);
    if (k == 13) mt9 = fmaxf(mt9, mtb), mtb = __shfl_xor(mt9, 32);
    if (k == 14) mt9 = fmaxf(mt9, mtb);
  };

#pragma unroll
  for (int i = 0; i < 36; ++i) sv[i] = -3.0e38f;
  mt = -3.0e38f; mt9 = -3.0e38f; m = 0.f; m9 = 0.f;
#pragma unroll 1
  for (int t = -1; t < 8; ++t) {
    STAMP(17 + 2 * (t));
    if (t < 0) WAITV(16); else WAITV(24);
    BARRIER();
    STAMP(18 + 2 * (t));
    if (t == 0) { m = mt; m9 = mt9; }
    else if (__any((mt - m > THR) || (mt9 - m9 > THR))) {
      const float mn = fmaxf(m, mt), mn9 = fmaxf(m9, mt9);
      const float al = __builtin_amdgcn_exp2f((m - mn) * CS), al9 = __builtin_amdgcn_exp2f((m9 - mn9) * CS);
      m = mn; m9 = mn9; l *= al; l9 *= al9;
#pragma unroll
      for (int i = 0; i < 128; ++i) ASCALE(i, al);
#pragma unroll
      for (int i = 128; i < 192; ++i) ASCALE(i, al9);
    }
    float mc = m * CS, mc9 = m9 * CS;
    unsigned pk[18];
    float xa[2][2], ea[2][2], lp0 = 0.f, lp1 = 0.f;
    auto sm1 = [&](int pr) {
      const float c_ = pr < 16 ? mc : mc9;
      float s0 = sv[2 * pr], s1 = sv[2 * pr + 1];
      asm volatile("" : "+v"(s0), "+v"(s1));
      xa[pr & 1][0] = fmaf(s0, CS, -c_); xa[pr & 1][1] = fmaf(s1, CS, -c_);
    };
    auto sm2 = [&](int pr) {
      asm volatile("v_exp_f32 %0, %2\n\tv_exp_f32 %1, %3" : "=&v"(ea[pr & 1][0]), "=&v"(ea[pr & 1][1]) : "v"(xa[pr & 1][0]), "v"(xa[pr & 1][1]));
    };
    auto sm3 = [&](int pr) {
      const float e0 = ea[pr & 1][0], e1 = ea[pr & 1][1];
      asm volatile("v_cvt_pk_bf16_f32 %0, %1, %2" : "=v"(pk[pr]) : "v"(e0), "v"(e1));
      if (pr < 16) { asm volatile("v_add_f32 %0, %0, %2\n\tv_add_f32 %1, %1, %3" : "+v"(lp0), "+v"(lp1) : "v"(e0), "v"(e1)); }
      else l9 += e0 + e1;
    };
    {
      if (t == 5) {
#pragma unroll
        for (int n = 0; n < 8; ++n) voffK[n] = 0;
      }
      const int ps = t < 0 ? 1 : PREVSL(sl);
      const char* gk = kg + (size_t)(t < 5 ? t + 3 : 0) * SLOT;
      s_phase([&](int k) {
        if ((k & 1) == 0 && (k >> 1) < 18) sm1(k >> 1);
        if ((k & 1) == 1 && (k >> 1) < 18) sm2(k >> 1);
        if ((k & 1) == 0 && k >= 2 && (k >> 1) - 1 < 18) sm3((k >> 1) - 1);
        if ((k & 3) == 1 && (k >> 2) < 8) pieceKV(gk, true, ps * SLOT, k >> 2);
      });
      l += lp0 + lp1;
    }
#pragma unroll
    for (int i = 0; i < 4; ++i) {
      u32x4 wv = {pk[4 * i], pk[4 * i + 1], pk[4 * i + 2], pk[4 * i + 3]};
      pf[i] = __builtin_bit_cast(bf16x8, wv);
    }
    {
      u32x2 wv = {pk[16], pk[17]};
      p9f = __builtin_bit_cast(bf16x4, wv);
    }
    if (t >= 0) {
      STAMP(35 + 2 * (t));
      WAITV(24);
      BARRIER();
      STAMP(36 + 2 * (t));
      bf16x8 fr[12];
      unsigned bV0 = lds_base + sl * SLOT + laneV; OPAQUE(bV0);
      const unsigned bV1 = (bV0 ^ 32) + 4096;
      unsigned bV9 = lds_base + sl * SLOT + laneV9; OPAQUE(bV9);
      auto ldf = [&](int k) -> bf16x8 {
        if (k < 32) {
          const int kp = k >> 3, d = k & 7;
          return trA2((bV0 ^ ((d & 3) << 6)) + (d >> 2) * 256 + kp * 8192, (bV1 ^ ((d & 3) << 6)) + (d >> 2) * 256 + kp * 8192);
        } else {
          const int d = k - 32;
          return trA2((bV9 ^ ((d & 3) << 6)) + (d >> 2) * 256, ((bV9 ^ 32) ^ ((d & 3) << 6)) + (d >> 2) * 256);
        }
      };
#pragma unroll
      for (int k = 0; k < 8; ++k) { const int kk = (k & ~3) + (3 - (k & 3)); fr[kk] = ldf(kk); }
      if (t == 6) {
#pragma unroll
        for (int n = 0; n < 8; ++n) voffV[n] = 0;
      }
      const int ps = PREVSL(sl);
      const char* gv = vg + (size_t)(t < 6 ? t + 2 : 0) * SLOT;
      SB();
#pragma unroll
      for (int k = 0; k < 40; ++k) {
        if (k < 32) M32(16 * (k & 7), fr[k % 12], pf[k >> 3]);
        else {
          const int d = k - 32;
          bf16x4 lo = __builtin_shufflevector(fr[k % 12], fr[k % 12], 0, 1, 2, 3), hi4 = __builtin_shufflevector(fr[k % 12], fr[k % 12], 4, 5, 6, 7);
          M16x16(128 + 8 * d, lo, p9f);
          M16x16(132 + 8 * d, hi4, p9f);
        }
        { const int kk = (k & ~3) + 8 + (3 - (k & 3)); if (kk < 40) fr[kk % 12] = ldf(kk); }
        if ((k & 3) == 1 && (k >> 2) < 8) pieceKV(gv, false, ps * SLOT, k >> 2);
        if (k >= 2 && k < 17) read_scores(k - 2);
        SB();
      }
      sl = NEXTSL(sl);
    } else {
      BARRIER();
#pragma unroll
      for (int n = 0; n < 8; ++n) pieceKV(vg + SLOT, false, 0, n);
      float dm = 0.f; (void)dm;
#pragma unroll
      for (int k = 0; k < 15; ++k) read_scores(k);
    }
  }
  WAITV(0);
  NOPS();
  l += __shfl_xor(l, 32);
  l9 += __shfl_xor(l9, 16);
  l9 += __shfl_xor(l9, 32);
  const float inv_l = 1.0f / l;

  STAMP(52);
  char* ofp = (char*)Of + (size_t)p * OF_PER_P * 2;
  {
    char* dst = ofp + ((size_t)(4 * b + w) * 256 + h * 16) * 1024 + lane * 16;
#pragma unroll
    for (int s = 0; s < 16; ++s) {
      const int A = 16 * (s >> 1) + 8 * (s & 1);
      bf16x8 v = pack8(AREAD(A) * inv_l, AREAD(A + 1) * inv_l, AREAD(A + 2) * inv_l, AREAD(A + 3) * inv_l,
                       AREAD(A + 4) * inv_l, AREAD(A + 5) * inv_l, AREAD(A + 6) * inv_l, AREAD(A + 7) * inv_l);
      *reinterpret_cast<bf16x8*>(dst + s * 1024) = v;
    }
  }
  RELANE();
  BARRIER();
  {
    float* st = reinterpret_cast<float*>(smem + 4 * 16 * 1040);
#pragma unroll
    for (int d = 0; d < 16; ++d) {
      f32x4 v = {AREAD(128 + 4 * d), AREAD(129 + 4 * d), AREAD(130 + 4 * d), AREAD(131 + 4 * d)};
      *reinterpret_cast<f32x4*>(smem + (w * 16 + i16) * 1040 + (16 * d + 4 * g) * 4) = v;
    }
    if (g == 0) { st[(w * 16 + i16) * 2] = m9 * CS; st[(w * 16 + i16) * 2 + 1] = l9; }
    BARRIER();
#pragma unroll
    for (int it = 0; it < 2; ++it) {
      const int item = tid + 256 * it;
      const int q = item >> 5, ck = item & 31;
      float mw[4], lw[4];
#pragma unroll
      for (int ww = 0; ww < 4; ++ww) { mw[ww] = st[(ww * 16 + q) * 2]; lw[ww] = st[(ww * 16 + q) * 2 + 1]; }
      const float M = fmaxf(fmaxf(mw[0], mw[1]), fmaxf(mw[2], mw[3]));
      float f[4], L = 0.f;
#pragma unroll
      for (int ww = 0; ww < 4; ++ww) { f[ww] = __builtin_amdgcn_exp2f(mw[ww] - M); L += f[ww] * lw[ww]; }
      const float il = 1.0f / L;
      float o[8];
#pragma unroll
      for (int j = 0; j < 8; ++j) o[j] = 0.f;
#pragma unroll
      for (int ww = 0; ww < 4; ++ww) {
        const f32x4 a = *reinterpret_cast<const f32x4*>(smem + (ww * 16 + q) * 1040 + ck * 32);
        const f32x4 c2 = *reinterpret_cast<const f32x4*>(smem + (ww * 16 + q) * 1040 + ck * 32 + 16);
#pragma unroll
        for (int j = 0; j < 4; ++j) { o[j] += f[ww] * a[j]; o[4 + j] += f[ww] * c2[j]; }
      }
      bf16x8 v = pack8(o[0] * il, o[1] * il, o[2] * il, o[3] * il, o[4] * il, o[5] * il, o[6] * il, o[7] * il);
      char* dst = ofp + ((size_t)(32 + (b >> 1)) * 256 + h * 16 + (ck >> 1)) * 1024 + (16 * (b & 1) + q + 32 * (ck & 1)) * 16;
      *reinterpret_cast<bf16x8*>(dst) = v;
    }
  }
  STAMP(53);
}

constexpr int K2_NBUF = 5;
constexpr int K2_WAVE_LDS = K2_NBUF * 4096;
DI bf16x8 gload16_asm(const char* p) {
  bf16x8 r;
  asm volatile("global_load_dwordx4 %0, %1, off" : "=v"(r) : "v"(p) : "memory");
  return r;
}
__global__ __launch_bounds__(512, 2) void oproj_kernel(const __hip_bfloat16* __restrict__ Of, const __hip_bfloat16* __restrict__ woA,
                                                       const __hip_bfloat16* __restrict__ woB, __hip_bfloat16* __restrict__ out) {
  extern __shared__ __attribute__((aligned(16))) char smem[];
  const int tid = threadIdx.x, lane = tid & 63;
  const int w = __builtin_amdgcn_readfirstlane(tid >> 6);
  const int T = (blockIdx.x & 7) * 32 + (blockIdx.x >> 3);
  if (T >= 252) return;
  const int p = T / 126, rem = T % 126, mg = rem / 7, ng = rem % 7;
  const int mgl = mg;
  const int ngl = ng;
  const char* of = (const char*)Of + (size_t)p * OF_PER_P * 2 + ((size_t)(2 * mgl) * 256 + w * 32) * 1024 + lane * 16;
  const char* wo = (const char*)(p ? woB : woA) + (size_t)(w * 512) * (DM * 2) + ngl * 128;
  const int hi = lane >> 5, blk = (lane >> 4) & 1, q4 = (lane & 15) >> 2, pp = lane & 3;
  const unsigned lds_base = (unsigned)(size_t)(lds_b4_ptr)smem + w * K2_WAVE_LDS;
  const unsigned voff = (unsigned)((lane >> 3) * (DM * 2) + (((lane & 7) ^ (((lane >> 4) & 1) << 2)) << 4));

  bf16x8 ob[K2_NBUF][2][2];
  auto issue = [&](int ck) {
#pragma unroll
    for (int mt = 0; mt < 2; ++mt)
#pragma unroll
      for (int ks = 0; ks < 2; ++ks) ob[ck % K2_NBUF][mt][ks] = gload16_asm(of + ((size_t)mt * 256 + 2 * ck + ks) * 1024);
#pragma unroll
    for (int n = 0; n < 4; ++n)
      glds16(wo + (size_t)(32 * ck + 8 * n) * (DM * 2), voff, lds_base + (ck % K2_NBUF) * 4096 + n * 1024);
  };
  f32x16 acc[2][2];
#pragma unroll
  for (int a = 0; a < 2; ++a)
#pragma unroll
    for (int c = 0; c < 2; ++c) acc[a][c] = f32x16{};
  issue(0); issue(1); issue(2); issue(3);
#pragma unroll
  for (int ck = 0; ck < 16; ++ck) {
    if (ck + 4 < 16) { issue(ck + 4); WAITV(32); }
    else if (ck == 12) WAITV(24); else if (ck == 13) WAITV(16); else if (ck == 14) WAITV(8); else WAITV(0);
    {
      bf16x8 (&o)[2][2] = ob[ck % K2_NBUF];
      asm volatile("" : "+v"(o[0][0]), "+v"(o[0][1]), "+v"(o[1][0]), "+v"(o[1][1]));
    }
    const unsigned sb = w * K2_WAVE_LDS + (ck % K2_NBUF) * 4096;
    bf16x8 fa[2][2];
#pragma unroll
    for (int ks = 0; ks < 2; ++ks)
#pragma unroll
      for (int nt = 0; nt < 2; ++nt) {
        const int row0 = 16 * ks + 8 * hi + q4, row1 = row0 + 4;
        const int ch = 4 * nt + 2 * blk + (pp >> 1);
        fa[ks][nt] = tr_read2(smem, sb + row0 * 128 + ((ch ^ (((row0 >> 1) & 1) << 2)) << 4) + 8 * (pp & 1),
                              sb + row1 * 128 + ((ch ^ (((row1 >> 1) & 1) << 2)) << 4) + 8 * (pp & 1));
      }
#pragma unroll
    for (int ks = 0; ks < 2; ++ks)
#pragma unroll
      for (int nt = 0; nt < 2; ++nt)
#pragma unroll
        for (int mt = 0; mt < 2; ++mt) acc[nt][mt] = mfma32(fa[ks][nt], ob[ck % K2_NBUF][mt][ks], acc[nt][mt]);
    asm volatile("s_waitcnt lgkmcnt(0)" ::: "memory");
  }
  __syncthreads();
  const int r32 = lane & 31;
#pragma unroll
  for (int nt = 0; nt < 2; ++nt)
#pragma unroll
    for (int mt = 0; mt < 2; ++mt)
#pragma unroll
      for (int qd = 0; qd < 4; ++qd) {
        f32x4 v = {acc[nt][mt][4 * qd], acc[nt][mt][4 * qd + 1], acc[nt][mt][4 * qd + 2], acc[nt][mt][4 * qd + 3]};
        *reinterpret_cast<f32x4*>(smem + ((size_t)w * 64 + 32 * mt + r32) * 272 + (32 * nt + 8 * qd + 4 * hi) * 4) = v;
      }
  __syncthreads();
  {
    const int m = tid >> 3, nc = tid & 7;
    float o[8];
#pragma unroll
    for (int j = 0; j < 8; ++j) o[j] = 0.f;
#pragma unroll
    for (int ww = 0; ww < 8; ++ww) {
      const f32x4 a = *reinterpret_cast<const f32x4*>(smem + ((size_t)ww * 64 + m) * 272 + nc * 32);
      const f32x4 c = *reinterpret_cast<const f32x4*>(smem + ((size_t)ww * 64 + m) * 272 + nc * 32 + 16);
#pragma unroll
      for (int j = 0; j < 4; ++j) { o[j] += a[j]; o[4 + j] += c[j]; }
    }
    const int mt = 2 * mg + (m >> 5), r = m & 31;
    int row;
    if (mt < 32) row = (mt >> 2) * SQ + (mt & 3) * 32 + r;
    else row = (2 * (mt - 32) + (r >> 4)) * SQ + 128 + (r & 15);
    bf16x8 v = pack8(o[0], o[1], o[2], o[3], o[4], o[5], o[6], o[7]);
    *reinterpret_cast<bf16x8*>((char*)out + ((size_t)row * (2 * DM) + p * DM + ng * 64 + nc * 8) * 2) = v;
  }
}

extern "C" void kernel_launch(void* const* d_in, const int* in_sizes, int n_in, void* d_out, int out_size, void* d_ws,
                              size_t ws_size, hipStream_t stream) {
  const __hip_bfloat16* xA = (const __hip_bfloat16*)d_in[0];
  const __hip_bfloat16* kA = (const __hip_bfloat16*)d_in[1];
  const __hip_bfloat16* vA = (const __hip_bfloat16*)d_in[2];
  const __hip_bfloat16* wqA = (const __hip_bfloat16*)d_in[3];
  const __hip_bfloat16* woA = (const __hip_bfloat16*)d_in[4];
  const __hip_bfloat16* xB = (const __hip_bfloat16*)d_in[5];
  const __hip_bfloat16* kB = (const __hip_bfloat16*)d_in[6];
  const __hip_bfloat16* vB = (const __hip_bfloat16*)d_in[7];
  const __hip_bfloat16* wqB = (const __hip_bfloat16*)d_in[8];
  const __hip_bfloat16* woB = (const __hip_bfloat16*)d_in[9];
  __hip_bfloat16* Of = (__hip_bfloat16*)d_ws;
  hipFuncSetAttribute((const void*)attn_kernel, hipFuncAttributeMaxDynamicSharedMemorySize, LDS_BYTES);
  hipFuncSetAttribute((const void*)oproj_kernel, hipFuncAttributeMaxDynamicSharedMemorySize, 8 * K2_WAVE_LDS);
  attn_kernel<<<256, 256, LDS_BYTES, stream>>>(xA, kA, vA, wqA, xB, kB, vB, wqB, Of, woA, woB
  );
  oproj_kernel<<<256, 512, 8 * K2_WAVE_LDS, stream>>>(Of, woA, woB, (__hip_bfloat16*)d_out);
}
```

```cpp
#include <hip/hip_runtime.h>
#include <hip/hip_bf16.h>
#include <stdint.h>
#include <type_traits>
#define ILQ 2
#define ILF 4

typedef short bf16x8 __attribute__((ext_vector_type(8)));
typedef short bf16x4 __attribute__((ext_vector_type(4)));
typedef float f32x16 __attribute__((ext_vector_type(16)));
typedef float f32x4 __attribute__((ext_vector_type(4)));
typedef unsigned u32x4 __attribute__((ext_vector_type(4)));
typedef unsigned u32x2 __attribute__((ext_vector_type(2)));
typedef __bf16 bf2_t __attribute__((ext_vector_type(2)));
typedef __attribute__((address_space(3))) bf16x4* lds_b4_ptr;

#define DI __device__ __forceinline__

constexpr int NB = 8, SQ = 144, DM = 448, NH = 16, SK = 512, DH = 256;
constexpr int SLOT = 32768;
constexpr int LDS_BYTES = 5 * SLOT;
constexpr size_t OF_PER_P = (size_t)36 * 256 * 512;

DI unsigned pack2(float a, float b) {
  bf2_t v = {(__bf16)a, (__bf16)b};
  return __builtin_bit_cast(unsigned, v);
}
DI bf16x8 pack8(float a0, float a1, float a2, float a3, float a4, float a5, float a6, float a7) {
  u32x4 w = {pack2(a0, a1), pack2(a2, a3), pack2(a4, a5), pack2(a6, a7)};
  return __builtin_bit_cast(bf16x8, w);
}
DI f32x16 mfma32(bf16x8 a, bf16x8 b, f32x16 c) { return __builtin_amdgcn_mfma_f32_32x32x16_bf16(a, b, c, 0, 0, 0); }
DI f32x4 mfma16x32(bf16x8 a, bf16x8 b, f32x4 c) { return __builtin_amdgcn_mfma_f32_16x16x32_bf16(a, b, c, 0, 0, 0); }
DI f32x4 mfma16x16(bf16x4 a, bf16x4 b, f32x4 c) { return __builtin_amdgcn_mfma_f32_16x16x16bf16_1k(a, b, c, 0, 0, 0); }

#define WAITV(n) asm volatile("s_waitcnt vmcnt(%0)" ::"n"(n) : "memory")
#define SB() __builtin_amdgcn_sched_barrier(0)
#define STAMP(i)
#define BARRIER() do { asm volatile("s_waitcnt lgkmcnt(0)" ::: "memory"); __builtin_amdgcn_s_barrier(); asm volatile("" ::: "memory"); } while (0)

DI bf16x4 tr_read(const char* smem, unsigned off) {
  return __builtin_amdgcn_ds_read_tr16_b64_v4i16((lds_b4_ptr)(smem + off));
}
DI bf16x8 tr_read2(const char* smem, unsigned off0, unsigned off1) {
  bf16x4 lo = tr_read(smem, off0), hi = tr_read(smem, off1);
  return __builtin_shufflevector(lo, hi, 0, 1, 2, 3, 4, 5, 6, 7);
}
DI void glds16(const char* gbase, unsigned voff, unsigned ldst) {
  unsigned keep;
  asm volatile("s_mov_b32 %0, m0\n\ts_mov_b32 m0, %3\n\ts_nop 0\n\tglobal_load_lds_dwordx4 %1, %2\n\ts_mov_b32 m0, %0"
               : "=&s"(keep) : "v"(voff), "s"(gbase), "s"(ldst) : "memory");
}
DI void glds16nt(const char* gbase, unsigned voff, unsigned ldst) {
  unsigned keep;
  asm volatile("s_mov_b32 %0, m0\n\ts_mov_b32 m0, %3\n\ts_nop 0\n\tglobal_load_lds_dwordx4 %1, %2 nt\n\ts_mov_b32 m0, %0"
               : "=&s"(keep) : "v"(voff), "s"(gbase), "s"(ldst) : "memory");
}
DI bf16x8 lds128(const char* smem, unsigned off) { return *reinterpret_cast<const bf16x8*>(smem + off); }

typedef __attribute__((address_space(3))) bf16x8* lds_b8_ptr;
DI bf16x4 trA(unsigned addr) { return __builtin_amdgcn_ds_read_tr16_b64_v4i16(reinterpret_cast<lds_b4_ptr>(addr)); }
DI bf16x8 trA2(unsigned a0, unsigned a1) {
  bf16x4 lo = trA(a0), hi = trA(a1);
  return __builtin_shufflevector(lo, hi, 0, 1, 2, 3, 4, 5, 6, 7);
}
DI bf16x8 ldA(unsigned addr) { return *reinterpret_cast<lds_b8_ptr>(addr); }
DI void ascale(float& a, float s) {
  float tmp;
  asm volatile("v_accvgpr_read_b32 %1, %0\n\tv_mul_f32 %1, %1, %2\n\tv_accvgpr_write_b32 %0, %1" : "+a"(a), "=&v"(tmp) : "v"(s));
}
DI void amfma32(f32x16& c, bf16x8 a, bf16x8 b) { asm volatile("v_mfma_f32_32x32x16_bf16 %0, %1, %2, %0" : "+a"(c) : "v"(a), "v"(b)); }
DI void amfma32z(f32x16& c, bf16x8 a, bf16x8 b) { asm volatile("v_mfma_f32_32x32x16_bf16 %0, %1, %2, 0" : "=a"(c) : "v"(a), "v"(b)); }
DI void amfma16x32(f32x4& c, bf16x8 a, bf16x8 b) { asm volatile("v_mfma_f32_16x16x32_bf16 %0, %1, %2, %0" : "+a"(c) : "v"(a), "v"(b)); }
DI void amfma16x32z(f32x4& c, bf16x8 a, bf16x8 b) { asm volatile("v_mfma_f32_16x16x32_bf16 %0, %1, %2, 0" : "=a"(c) : "v"(a), "v"(b)); }
DI void amfma16x16(f32x4& c, bf16x4 a, bf16x4 b) { asm volatile("v_mfma_f32_16x16x16_bf16 %0, %1, %2, %0" : "+a"(c) : "v"(a), "v"(b)); }
#define MFMA_DRAIN() asm volatile("s_nop 15\n\ts_nop 15" ::: "memory")
#define OPAQUE(x) asm volatile("" : "+v"(x))

DI int swzT(int row) { return ((row & 3) << 2) | ((((row >> 2) ^ (row >> 3)) & 1) << 1); }
DI int swap2(int p) { return ((p & 1) << 1) | (p >> 1); }

constexpr int STG = 53248;

#define ALL_AGPRS "a0", "a1", "a2", "a3", "a4", "a5", "a6", "a7", "a8", "a9", "a10", "a11", "a12", "a13", "a14", "a15", "a16", "a17", "a18", "a19", "a20", "a21", "a22", "a23", "a24", "a25", "a26", "a27", "a28", "a29", "a30", "a31", "a32", "a33", "a34", "a35", "a36", "a37", "a38", "a39", "a40", "a41", "a42", "a43", "a44", "a45", "a46", "a47", "a48", "a49", "a50", "a51", "a52", "a53", "a54", "a55", "a56", "a57", "a58", "a59", "a60", "a61", "a62", "a63", "a64", "a65", "a66", "a67", "a68", "a69", "a70", "a71", "a72", "a73", "a74", "a75", "a76", "a77", "a78", "a79", "a80", "a81", "a82", "a83", "a84", "a85", "a86", "a87", "a88", "a89", "a90", "a91", "a92", "a93", "a94", "a95", "a96", "a97", "a98", "a99", "a100", "a101", "a102", "a103", "a104", "a105", "a106", "a107", "a108", "a109", "a110", "a111", "a112", "a113", "a114", "a115", "a116", "a117", "a118", "a119", "a120", "a121", "a122", "a123", "a124", "a125", "a126", "a127", "a128", "a129", "a130", "a131", "a132", "a133", "a134", "a135", "a136", "a137", "a138", "a139", "a140", "a141", "a142", "a143", "a144", "a145", "a146", "a147", "a148", "a149", "a150", "a151", "a152", "a153", "a154", "a155", "a156", "a157", "a158", "a159", "a160", "a161", "a162", "a163", "a164", "a165", "a166", "a167", "a168", "a169", "a170", "a171", "a172", "a173", "a174", "a175", "a176", "a177", "a178", "a179", "a180", "a181", "a182", "a183", "a184", "a185", "a186", "a187", "a188", "a189", "a190", "a191", "a192", "a193", "a194", "a195", "a196", "a197", "a198", "a199", "a200", "a201", "a202", "a203", "a204", "a205", "a206", "a207", "a208", "a209", "a210", "a211", "a212", "a213", "a214", "a215", "a216", "a217", "a218", "a219", "a220", "a221", "a222", "a223", "a224", "a225", "a226", "a227", "a228", "a229", "a230", "a231", "a232", "a233", "a234", "a235", "a236", "a237", "a238", "a239", "a240", "a241", "a242", "a243", "a244", "a245", "a246", "a247", "a248", "a249", "a250", "a251", "a252", "a253", "a254", "a255"
DI void M32(int A, bf16x8 a, bf16x8 b) { asm volatile("v_mfma_f32_32x32x16_bf16 a[%c2:%c3], %0, %1, a[%c2:%c3]" ::"v"(a), "v"(b), "i"(A), "i"(A + 15) : ALL_AGPRS); }
DI void M32Z(int A, bf16x8 a, bf16x8 b) { asm volatile("v_mfma_f32_32x32x16_bf16 a[%c2:%c3], %0, %1, 0" ::"v"(a), "v"(b), "i"(A), "i"(A + 15) : ALL_AGPRS); }
DI void M16x32(int A, bf16x8 a, bf16x8 b) { asm volatile("v_mfma_f32_16x16x32_bf16 a[%c2:%c3], %0, %1, a[%c2:%c3]" ::"v"(a), "v"(b), "i"(A), "i"(A + 3) : ALL_AGPRS); }
DI void M16x32Z(int A, bf16x8 a, bf16x8 b) { asm volatile("v_mfma_f32_16x16x32_bf16 a[%c2:%c3], %0, %1, 0" ::"v"(a), "v"(b), "i"(A), "i"(A + 3) : ALL_AGPRS); }
DI void M16x16(int A, bf16x4 a, bf16x4 b) { asm volatile("v_mfma_f32_16x16x16_bf16 a[%c2:%c3], %0, %1, a[%c2:%c3]" ::"v"(a), "v"(b), "i"(A), "i"(A + 3) : ALL_AGPRS); }
DI float AREAD(int A) { float r; asm volatile("v_accvgpr_read_b32 %0, a%c1" : "=v"(r) : "i"(A)); return r; }
DI void AZERO(int A) { asm volatile("v_accvgpr_write_b32 a%c0, 0" ::"i"(A) : ALL_AGPRS); }
DI void ASCALE(int A, float s) {
  float t;
  asm volatile("v_accvgpr_read_b32 %0, a%c2\n\tv_mul_f32 %0, %0, %1\n\tv_accvgpr_write_b32 a%c2, %0" : "=&v"(t) : "v"(s), "i"(A));
}
#define NOPS() asm volatile("s_nop 15\n\ts_nop 15")
template <int OFF, bool NT>
DI void dma(const char* gbase, unsigned voff, unsigned ldst) {
  if (NT) asm volatile("s_mov_b32 m0, %2\n\ts_nop 0\n\tglobal_load_lds_dwordx4 %0, %1 offset:%c3 nt" ::"v"(voff), "s"(gbase), "s"(ldst), "i"(OFF) : "memory");
  else asm volatile("s_mov_b32 m0, %2\n\ts_nop 0\n\tglobal_load_lds_dwordx4 %0, %1 offset:%c3" ::"v"(voff), "s"(gbase), "s"(ldst), "i"(OFF) : "memory");
}

template <bool NT>
DI void dmap(const char* gbase, unsigned voff, unsigned ldst, int flag) {
  asm volatile("s_cmp_eq_u32 %3, 0\n\ts_cbranch_scc1 1f\n\ts_mov_b32 m0, %2\n\ts_nop 0\n\tglobal_load_lds_dwordx4 %0, %1\n1:"
               ::"v"(voff), "s"(gbase), "s"(ldst), "s"(flag) : "memory", "scc");
}
DI void zero_acc() {
  bf16x8 z = {};
  asm volatile("" : "+v"(z));
#pragma unroll
  for (int i = 0; i < 12; ++i) M32Z(16 * i, z, z);
}

__global__ __launch_bounds__(256, 1) void attn_kernel(
    const __hip_bfloat16* __restrict__ xA, const __hip_bfloat16* __restrict__ kA, const __hip_bfloat16* __restrict__ vA,
    const __hip_bfloat16* __restrict__ wqA, const __hip_bfloat16* __restrict__ xB, const __hip_bfloat16* __restrict__ kB,
    const __hip_bfloat16* __restrict__ vB, const __hip_bfloat16* __restrict__ wqB, __hip_bfloat16* __restrict__ Of,
    const __hip_bfloat16* __restrict__ woA, const __hip_bfloat16* __restrict__ woB
    ) {
  extern __shared__ __attribute__((aligned(16))) char smem[];
  asm volatile("" ::: "a0", "a64", "a128", "a192", "a227");
  const int tid = threadIdx.x, lane = tid & 63;
  const int w = __builtin_amdgcn_readfirstlane(tid >> 6);
  const int bid = blockIdx.x, xcd = bid & 7, idx = bid >> 3;
  const int ph = xcd * 4 + (idx >> 3), p = ph >> 4, h = ph & 15, b = idx & 7;
  const char* xg = (const char*)(p ? xB : xA) + (size_t)b * SQ * DM * 2;
  const char* wg = (const char*)(p ? wqB : wqA) + (size_t)h * DH * 2;
  const char* kg = (const char*)(p ? kB : kA) + (size_t)(b * NH + h) * SK * DH * 2;
  const char* vg = (const char*)(p ? vB : vA) + (size_t)(b * NH + h) * SK * DH * 2;

  int r32 = lane & 31, hi = lane >> 5, i16 = lane & 15, g = lane >> 4;
  int blk = (lane >> 4) & 1, q4 = (lane & 15) >> 2, pp = lane & 3;
#define RELANE() do { int l_ = lane; asm volatile("" : "+v"(l_)); r32 = l_ & 31; hi = l_ >> 5; i16 = l_ & 15; g = l_ >> 4; \
    blk = (l_ >> 4) & 1; q4 = (l_ & 15) >> 2; pp = l_ & 3; } while (0)

  const unsigned lds_base = (unsigned)(size_t)(lds_b4_ptr)smem;
  unsigned voffK[8], voffV[8], voffW[8], voffX[5];
#pragma unroll
  for (int n = 0; n < 8; ++n) {
    const unsigned xk = (unsigned)(2 * n), xv = (unsigned)((((2 * n) & 3) << 2) | ((((n >> 1) ^ (n >> 2)) & 1) << 1));
    voffK[n] = (unsigned)((16 * w + hi) * 512 + (((r32 ^ hi) ^ xk) << 4));
    voffV[n] = (unsigned)((16 * w + hi) * 512 + (((r32 ^ (hi << 2)) ^ xv) << 4));
    voffW[n] = (unsigned)((16 * w + hi + 2 * n) * 8192 + (((r32 ^ (hi << 2)) ^ xv) << 4));
  }
#pragma unroll
  for (int n = 0; n < 5; ++n) {
    const int gi = 5 * w + n;
    int row = 8 * gi + (lane >> 3);
    const int ch = (lane & 7) ^ ((row >> 1) & 7);
    row = row < SQ ? row : SQ - 1;
    voffX[n] = (unsigned)(row * (DM * 2) + ch * 16);
  }
  const unsigned ldsw = lds_base + w * 8192;
  auto pieceKV = [&](const char* gb, bool isK, unsigned ldst, int n) {
    const unsigned vo = isK ? voffK[n] : voffV[n];
    const char* g2 = gb + (n >> 2) * 4096;
    const unsigned l2 = ldsw + ldst + (n >> 2) * 4096;
    switch (n & 3) {
      case 0: dma<0, true>(g2, vo, l2); break;
      case 1: dma<1024, true>(g2, vo, l2); break;
      case 2: dma<2048, true>(g2, vo, l2); break;
      default: dma<3072, true>(g2, vo, l2); break;
    }
  };
  auto pieceQ = [&](const char* wgc, const char* xgc, unsigned ldst, int n) {
    if (n < 8) dma<0, false>(wgc, voffW[n], ldsw + ldst + n * 1024);
    else dma<0, false>(xgc, voffX[n - 8], lds_base + ldst + SLOT + (5 * w + n - 8) * 1024);
  };
  STAMP(0);
#pragma unroll
  for (int n = 0; n < 13; ++n) pieceQ(wg, xg, 0, n);
#pragma unroll
  for (int n = 0; n < 13; ++n) pieceQ(wg + 64 * NH * DH * 2, xg + 128, STG, n);
  {
    const char* wop = (const char*)(p ? woB : woA) + (size_t)(b * NH + h) * 28672 + w * 7168;
#pragma unroll
    for (int n = 0; n < 7; ++n) dma<0, false>(wop + n * 1024, (unsigned)(lane * 16), lds_base + 3 * STG + w * 1024);
  }
  zero_acc();

  {
    const int spw = swap2(pp);
    const unsigned laneW = (unsigned)((8 * hi + q4) * 512 + ((((q4 << 2) | ((blk ^ hi) << 1)) ^ (spw >> 1)) << 4) + 8 * (spw & 1));
    const unsigned lane9 = (unsigned)((8 * g + q4) * 512 + (((8 * w + (pp >> 1)) ^ ((q4 << 2) | ((g & 1) << 1))) << 4) + 8 * (pp & 1));
    const unsigned laneX = (unsigned)(SLOT + (32 * w + r32) * 128 + ((hi ^ ((r32 >> 1) & 7)) << 4));
    const unsigned laneX9 = (unsigned)(SLOT + (128 + i16) * 128 + ((g ^ ((i16 >> 1) & 7)) << 4));
    unsigned stg = 0;
#pragma unroll 1
    for (int c = 0; c < 7; ++c) {
      STAMP(1 + 2 * c);
      if (c < 2) WAITV(20); else if (c < 6) WAITV(13); else WAITV(8);
      BARRIER();
      STAMP(2 + 2 * c);
      bf16x8 fr[12], xq[4], x9q[2];
      unsigned bW0 = lds_base + stg + laneW; OPAQUE(bW0);
      unsigned bX = lds_base + stg + laneX; OPAQUE(bX);
      unsigned bX9 = lds_base + stg + laneX9; OPAQUE(bX9);
      const unsigned bW1 = (bW0 ^ 32) + 2048;
      unsigned b9 = lds_base + stg + lane9; OPAQUE(b9);
      auto ldf = [&](int k) -> bf16x8 {
        if (k < 32) {
          const int ks = k >> 3, d = k & 7;
          return trA2((bW0 ^ ((d & 3) << 6)) + (d >> 2) * 256 + ks * 8192, (bW1 ^ ((d & 3) << 6)) + (d >> 2) * 256 + ks * 8192);
        } else {
          const int ks = (k - 32) >> 2, d = (k - 32) & 3;
          return trA2((b9 ^ (d << 5)) + ks * 16384, ((b9 ^ 32) ^ (d << 5)) + 2048 + ks * 16384);
        }
      };
#pragma unroll
      for (int ks = 0; ks < 4; ++ks) xq[ks] = ldA(bX ^ (ks << 5));
#pragma unroll
      for (int ks = 0; ks < 2; ++ks) x9q[ks] = ldA(bX9 ^ (ks << 6));
#pragma unroll
      for (int k = 0; k < 8; ++k) { const int kk = (k & ~3) + (3 - (k & 3)); fr[kk] = ldf(kk); }
      unsigned nx = stg + 2 * STG; nx = nx >= 3 * STG ? nx - 3 * STG : nx;
      const char* wgc = wg + (size_t)(c + 2) * (64 * NH * DH * 2);
      const char* xgc = xg + (c + 2) * 128;
      if (c == 5) {
#pragma unroll
        for (int n = 0; n < 8; ++n) pieceKV(kg, true, 2 * SLOT, n);
      } else if (c == 6) {
#pragma unroll
        for (int n = 0; n < 8; ++n) pieceKV(kg + SLOT, true, 3 * SLOT, n);
#pragma unroll
        for (int n = 0; n < 8; ++n) pieceKV(vg, false, 4 * SLOT, n);
      }
      const int fq = __builtin_amdgcn_readfirstlane(c <= 4);
      SB();
#pragma unroll
      for (int k = 0; k < 40; ++k) {
        if (k < 32) M32(16 * (k & 7), fr[k % 12], xq[k >> 3]);
        else M16x32(128 + 4 * ((k - 32) & 3), fr[k % 12], x9q[(k - 32) >> 2]);
        { const int kk = (k & ~3) + 8 + (3 - (k & 3)); if (kk < 40) fr[kk % 12] = ldf(kk); }
        if ((k % 3) == 0 && k / 3 < 13) {
          const int n = k / 3;
          if (n < 8) dmap<false>(wgc, voffW[n], ldsw + nx + n * 1024, fq);
          else dmap<false>(xgc, voffX[n - 8], lds_base + nx + SLOT + (5 * w + n - 8) * 1024, fq);
        }
        SB();
      }
      stg += STG; stg = stg >= 3 * STG ? 0 : stg;
    }
  }
  NOPS();
  bf16x8 Qf[16];
#pragma unroll
  for (int s = 0; s < 16; ++s) {
    const int A = 16 * (s >> 1) + 8 * (s & 1);
    Qf[s] = pack8(AREAD(A), AREAD(A + 1), AREAD(A + 2), AREAD(A + 3), AREAD(A + 4), AREAD(A + 5), AREAD(A + 6), AREAD(A + 7));
  }
  RELANE();
  BARRIER();
#pragma unroll
  for (int d = 0; d < 4; ++d) {
    u32x2 v = {pack2(AREAD(128 + 4 * d), AREAD(129 + 4 * d)), pack2(AREAD(130 + 4 * d), AREAD(131 + 4 * d))};
    const int dh = 64 * w + 16 * d + 4 * g;
    *reinterpret_cast<u32x2*>(smem + SLOT + i16 * 512 + (((dh >> 3) ^ i16) << 4) + ((dh & 4) << 1)) = v;
  }
  zero_acc();
  bf16x8 Q9f[8];
  BARRIER();
#pragma unroll
  for (int s = 0; s < 8; ++s) Q9f[s] = ldA(lds_base + SLOT + i16 * 512 + (((4 * s + g) ^ i16) << 4));

  constexpr float CS = 0.0625f * 1.4426950408889634f;
  constexpr float THR = 8.0f / CS;
  float m = -3.0e38f, l = 0.f, m9 = -3.0e38f, l9 = 0.f;
  const unsigned laneK = (unsigned)(r32 * 512 + ((hi ^ (r32 & 15)) << 4));
  const unsigned laneK9 = (unsigned)((16 * w + i16) * 512 + ((g ^ i16) << 4));
  const int spv = swap2(pp);
  const unsigned laneV = (unsigned)((4 * hi + q4) * 512 + ((((q4 << 2) | ((blk ^ hi) << 1)) ^ (spv >> 1)) << 4) + 8 * (spv & 1));
  const unsigned laneV9 = (unsigned)((16 * w + 4 * g + q4) * 512 + (((pp >> 1) ^ ((q4 << 2) | (((g ^ (g >> 1)) & 1) << 1))) << 4) + 8 * (pp & 1));
#define NEXTSL(x) ((x) == 4 ? 1 : (x) == 1 ? 0 : (x) == 0 ? 2 : (x) + 1)
#define PREVSL(x) ((x) == 1 ? 4 : (x) == 0 ? 1 : (x) == 2 ? 0 : (x) - 1)
  int sl = 2;
  float sv[36];
  float mt, mt9;
  bf16x8 pf[4];
  bf16x4 p9f;

  auto s_phase = [&](auto&& extra) {
    bf16x8 fr[12];
    unsigned bK = lds_base + sl * SLOT + laneK; OPAQUE(bK);
    unsigned bK9 = lds_base + sl * SLOT + laneK9; OPAQUE(bK9);
    auto ldf = [&](int k) -> bf16x8 {
      if (k < 32) return ldA((bK ^ ((k & 7) << 5)) + ((k >> 3) & 1) * 256 + (k >> 4) * 16384);
      else return ldA((bK9 ^ ((k & 3) << 6)) + ((k >> 2) & 1) * 256);
    };
#pragma unroll
    for (int k = 0; k < 8; ++k) { const int kk = (k & ~3) + (3 - (k & 3)); fr[kk] = ldf(kk); }
    SB();
#pragma unroll
    for (int k = 0; k < 40; ++k) {
      if (k < 32) { if ((k & 15) == 0) M32Z(192 + 16 * (k >> 4), fr[k % 12], Qf[k & 15]); else M32(192 + 16 * (k >> 4), fr[k % 12], Qf[k & 15]); }
      else { if (k == 32) M16x32Z(224, fr[k % 12], Q9f[k - 32]); else M16x32(224, fr[k % 12], Q9f[k - 32]); }
      { const int kk = (k & ~3) + 8 + (3 - (k & 3)); if (kk < 40) fr[kk % 12] = ldf(kk); }
      extra(k);
      SB();
    }
    sl = NEXTSL(sl);
  };
  float mtb;
  auto read_scores = [&](int k) {
    if (k < 9) {
#pragma unroll
      for (int i = 0; i < 4; ++i) sv[4 * k + i] = AREAD(192 + 4 * k + i);
    }
    if (k == 1) {
      asm volatile("v_max_f32 %0, %2, %3\n\tv_max_f32 %1, %4, %5" : "=&v"(mt), "=&v"(mtb) : "v"(sv[0]), "v"(sv[1]), "v"(sv[2]), "v"(sv[3]));
    } else if (k >= 2 && k < 9) {
      const int j = 4 * (k - 1);
      asm volatile("v_max3_f32 %0, %0, %2, %3\n\tv_max3_f32 %1, %1, %4, %5" : "+v"(mt), "+v"(mtb) : "v"(sv[j]), "v"(sv[j + 1]), "v"(sv[j + 2]), "v"(sv[j + 3]));
    }
    if (k == 9) {
      asm volatile("v_max_f32 %0, %0, %2\n\tv_max3_f32 %1, %3, %4, %5" : "+v"(mt), "=&v"(mt9) : "v"(mtb), "v"(sv[32]), "v"(sv[33]), "v"(sv[34]));
    }
    if (k == 10) {
      asm volatile("v_max_f32 %0, %0, %1" : "+v"(mt9) : "v"(sv[35]));
      mtb = __shfl_xor(mt, 32);
    }
    if (k == 11) mtb = fmaxf(mt, mtb), mt = mtb, mtb = __shfl_xor(mt9, 16);
    if (k == 13) mt9 = fmaxf(mt9, mtb), mtb = __shfl_xor(mt9, 32);
    if (k == 14) mt9 = fmaxf(mt9, mtb);
  };

#pragma unroll
  for (int i = 0; i < 36; ++i) sv[i] = -3.0e38f;
  mt = -3.0e38f; mt9 = -3.0e38f; m = 0.f; m9 = 0.f;
#pragma unroll 1
  for (int t = -1; t < 8; ++t) {
    STAMP(17 + 2 * (t));
    if (t < 0) WAITV(16); else WAITV(24);
    BARRIER();
    STAMP(18 + 2 * (t));
    if (t == 0) { m = mt; m9 = mt9; }
    else if (__any((mt - m > THR) || (mt9 - m9 > THR))) {
      const float mn = fmaxf(m, mt), mn9 = fmaxf(m9, mt9);
      const float al = __builtin_amdgcn_exp2f((m - mn) * CS), al9 = __builtin_amdgcn_exp2f((m9 - mn9) * CS);
      m = mn; m9 = mn9; l *= al; l9 *= al9;
#pragma unroll
      for (int i = 0; i < 128; ++i) ASCALE(i, al);
#pragma unroll
      for (int i = 128; i < 192; ++i) ASCALE(i, al9);
    }
    float mc = m * CS, mc9 = m9 * CS;
    unsigned pk[18];
    float xa[2][2], ea[2][2], lp0 = 0.f, lp1 = 0.f;
    auto sm1 = [&](int pr) {
      const float c_ = pr < 16 ? mc : mc9;
      float s0 = sv[2 * pr], s1 = sv[2 * pr + 1];
      asm volatile("" : "+v"(s0), "+v"(s1));
      xa[pr & 1][0] = fmaf(s0, CS, -c_); xa[pr & 1][1] = fmaf(s1, CS, -c_);
    };
    auto sm2 = [&](int pr) {
      asm volatile("v_exp_f32 %0, %2\n\tv_exp_f32 %1, %3" : "=&v"(ea[pr & 1][0]), "=&v"(ea[pr & 1][1]) : "v"(xa[pr & 1][0]), "v"(xa[pr & 1][1]));
    };
    auto sm3 = [&](int pr) {
      const float e0 = ea[pr & 1][0], e1 = ea[pr & 1][1];
      asm volatile("v_cvt_pk_bf16_f32 %0, %1, %2" : "=v"(pk[pr]) : "v"(e0), "v"(e1));
      if (pr < 16) { asm volatile("v_add_f32 %0, %0, %2\n\tv_add_f32 %1, %1, %3" : "+v"(lp0), "+v"(lp1) : "v"(e0), "v"(e1)); }
      else l9 += e0 + e1;
    };
    {
      if (t == 5) {
#pragma unroll
        for (int n = 0; n < 8; ++n) voffK[n] = 0;
      }
      const int ps = t < 0 ? 1 : PREVSL(sl);
      const char* gk = kg + (size_t)(t < 5 ? t + 3 : 0) * SLOT;
      s_phase([&](int k) {
        if ((k & 1) == 0 && (k >> 1) < 18) sm1(k >> 1);
        if ((k & 1) == 1 && (k >> 1) < 18) sm2(k >> 1);
        if ((k & 1) == 0 && k >= 2 && (k >> 1) - 1 < 18) sm3((k >> 1) - 1);
        if ((k & 3) == 1 && (k >> 2) < 8) pieceKV(gk, true, ps * SLOT, k >> 2);
      });
      l += lp0 + lp1;
    }
#pragma unroll
    for (int i = 0; i < 4; ++i) {
      u32x4 wv = {pk[4 * i], pk[4 * i + 1], pk[4 * i + 2], pk[4 * i + 3]};
      pf[i] = __builtin_bit_cast(bf16x8, wv);
    }
    {
      u32x2 wv = {pk[16], pk[17]};
      p9f = __builtin_bit_cast(bf16x4, wv);
    }
    if (t >= 0) {
      STAMP(35 + 2 * (t));
      WAITV(24);
      BARRIER();
      STAMP(36 + 2 * (t));
      bf16x8 fr[12];
      unsigned bV0 = lds_base + sl * SLOT + laneV; OPAQUE(bV0);
      const unsigned bV1 = (bV0 ^ 32) + 4096;
      unsigned bV9 = lds_base + sl * SLOT + laneV9; OPAQUE(bV9);
      auto ldf = [&](int k) -> bf16x8 {
        if (k < 32) {
          const int kp = k >> 3, d = k & 7;
          return trA2((bV0 ^ ((d & 3) << 6)) + (d >> 2) * 256 + kp * 8192, (bV1 ^ ((d & 3) << 6)) + (d >> 2) * 256 + kp * 8192);
        } else {
          const int d = k - 32;
          return trA2((bV9 ^ ((d & 3) << 6)) + (d >> 2) * 256, ((bV9 ^ 32) ^ ((d & 3) << 6)) + (d >> 2) * 256);
        }
      };
#pragma unroll
      for (int k = 0; k < 8; ++k) { const int kk = (k & ~3) + (3 - (k & 3)); fr[kk] = ldf(kk); }
      if (t == 6) {
#pragma unroll
        for (int n = 0; n < 8; ++n) voffV[n] = 0;
      }
      const int ps = PREVSL(sl);
      const char* gv = vg + (size_t)(t < 6 ? t + 2 : 0) * SLOT;
      SB();
#pragma unroll
      for (int k = 0; k < 40; ++k) {
        if (k < 32) M32(16 * (k & 7), fr[k % 12], pf[k >> 3]);
        else {
          const int d = k - 32;
          bf16x4 lo = __builtin_shufflevector(fr[k % 12], fr[k % 12], 0, 1, 2, 3), hi4 = __builtin_shufflevector(fr[k % 12], fr[k % 12], 4, 5, 6, 7);
          M16x16(128 + 8 * d, lo, p9f);
          M16x16(132 + 8 * d, hi4, p9f);
        }
        { const int kk = (k & ~3) + 8 + (3 - (k & 3)); if (kk < 40) fr[kk % 12] = ldf(kk); }
        if ((k & 3) == 1 && (k >> 2) < 8) pieceKV(gv, false, ps * SLOT, k >> 2);
        if (k >= 2 && k < 17) read_scores(k - 2);
        SB();
      }
      sl = NEXTSL(sl);
    } else {
      BARRIER();
#pragma unroll
      for (int n = 0; n < 8; ++n) pieceKV(vg + SLOT, false, 0, n);
      float dm = 0.f; (void)dm;
#pragma unroll
      for (int k = 0; k < 15; ++k) read_scores(k);
    }
  }
  WAITV(0);
  NOPS();
  l += __shfl_xor(l, 32);
  l9 += __shfl_xor(l9, 16);
  l9 += __shfl_xor(l9, 32);
  const float inv_l = 1.0f / l;

  STAMP(52);
  char* ofp = (char*)Of + (size_t)p * OF_PER_P * 2;
  {
    char* dst = ofp + ((size_t)(4 * b + w) * 256 + h * 16) * 1024 + lane * 16;
#pragma unroll
    for (int s = 0; s < 16; ++s) {
      const int A = 16 * (s >> 1) + 8 * (s & 1);
      bf16x8 v = pack8(AREAD(A) * inv_l, AREAD(A + 1) * inv_l, AREAD(A + 2) * inv_l, AREAD(A + 3) * inv_l,
                       AREAD(A + 4) * inv_l, AREAD(A + 5) * inv_l, AREAD(A + 6) * inv_l, AREAD(A + 7) * inv_l);
      *reinterpret_cast<bf16x8*>(dst + s * 1024) = v;
    }
  }
  RELANE();
  BARRIER();
  {
    float* st = reinterpret_cast<float*>(smem + 4 * 16 * 1040);
#pragma unroll
    for (int d = 0; d < 16; ++d) {
      f32x4 v = {AREAD(128 + 4 * d), AREAD(129 + 4 * d), AREAD(130 + 4 * d), AREAD(131 + 4 * d)};
      *reinterpret_cast<f32x4*>(smem + (w * 16 + i16) * 1040 + (16 * d + 4 * g) * 4) = v;
    }
    if (g == 0) { st[(w * 16 + i16) * 2] = m9 * CS; st[(w * 16 + i16) * 2 + 1] = l9; }
    BARRIER();
#pragma unroll
    for (int it = 0; it < 2; ++it) {
      const int item = tid + 256 * it;
      const int q = item >> 5, ck = item & 31;
      float mw[4], lw[4];
#pragma unroll
      for (int ww = 0; ww < 4; ++ww) { mw[ww] = st[(ww * 16 + q) * 2]; lw[ww] = st[(ww * 16 + q) * 2 + 1]; }
      const float M = fmaxf(fmaxf(mw[0], mw[1]), fmaxf(mw[2], mw[3]));
      float f[4], L = 0.f;
#pragma unroll
      for (int ww = 0; ww < 4; ++ww) { f[ww] = __builtin_amdgcn_exp2f(mw[ww] - M); L += f[ww] * lw[ww]; }
      const float il = 1.0f / L;
      float o[8];
#pragma unroll
      for (int j = 0; j < 8; ++j) o[j] = 0.f;
#pragma unroll
      for (int ww = 0; ww < 4; ++ww) {
        const f32x4 a = *reinterpret_cast<const f32x4*>(smem + (ww * 16 + q) * 1040 + ck * 32);
        const f32x4 c2 = *reinterpret_cast<const f32x4*>(smem + (ww * 16 + q) * 1040 + ck * 32 + 16);
#pragma unroll
        for (int j = 0; j < 4; ++j) { o[j] += f[ww] * a[j]; o[4 + j] += f[ww] * c2[j]; }
      }
      bf16x8 v = pack8(o[0] * il, o[1] * il, o[2] * il, o[3] * il, o[4] * il, o[5] * il, o[6] * il, o[7] * il);
      char* dst = ofp + ((size_t)(32 + (b >> 1)) * 256 + h * 16 + (ck >> 1)) * 1024 + (16 * (b & 1) + q + 32 * (ck & 1)) * 16;
      *reinterpret_cast<bf16x8*>(dst) = v;
    }
  }
  STAMP(53);
}

constexpr int K2_NBUF = 5;
constexpr int K2_WAVE_LDS = K2_NBUF * 4096;
DI bf16x8 gload16_asm(const char* p) {
  bf16x8 r;
  asm volatile("global_load_dwordx4 %0, %1, off" : "=v"(r) : "v"(p) : "memory");
  return r;
}
__global__ __launch_bounds__(512, 2) void oproj_kernel(const __hip_bfloat16* __restrict__ Of, const __hip_bfloat16* __restrict__ woA,
                                                       const __hip_bfloat16* __restrict__ woB, __hip_bfloat16* __restrict__ out) {
  extern __shared__ __attribute__((aligned(16))) char smem[];
  const int tid = threadIdx.x, lane = tid & 63;
  const int w = __builtin_amdgcn_readfirstlane(tid >> 6);
  const int T = (blockIdx.x & 7) * 32 + (blockIdx.x >> 3);
  if (T >= 252) return;
  const int p = T / 126, rem = T % 126, mg = rem / 7, ng = rem % 7;
  const char* of = (const char*)Of + (size_t)p * OF_PER_P * 2 + ((size_t)(2 * mg) * 256 + w * 32) * 1024 + lane * 16;
  const char* wo = (const char*)(p ? woB : woA) + (size_t)(w * 512) * (DM * 2) + ng * 128;
  const int hi = lane >> 5, blk = (lane >> 4) & 1, q4 = (lane & 15) >> 2, pp = lane & 3;
  const unsigned lds_base = (unsigned)(size_t)(lds_b4_ptr)smem + w * K2_WAVE_LDS;
  const unsigned voff = (unsigned)((lane >> 3) * (DM * 2) + (((lane & 7) ^ (((lane >> 4) & 1) << 2)) << 4));

  bf16x8 ob[K2_NBUF][2][2];
  auto issue = [&](int ck) {
#pragma unroll
    for (int mt = 0; mt < 2; ++mt)
#pragma unroll
      for (int ks = 0; ks < 2; ++ks) ob[ck % K2_NBUF][mt][ks] = gload16_asm(of + ((size_t)mt * 256 + 2 * ck + ks) * 1024);
#pragma unroll
    for (int n = 0; n < 4; ++n)
      glds16(wo + (size_t)(32 * ck + 8 * n) * (DM * 2), voff, lds_base + (ck % K2_NBUF) * 4096 + n * 1024);
  };
  f32x16 acc[2][2];
#pragma unroll
  for (int a = 0; a < 2; ++a)
#pragma unroll
    for (int c = 0; c < 2; ++c) acc[a][c] = f32x16{};
  issue(0); issue(1); issue(2); issue(3);
#pragma unroll
  for (int ck = 0; ck < 16; ++ck) {
    if (ck + 4 < 16) { issue(ck + 4); WAITV(32); }
    else if (ck == 12) WAITV(24); else if (ck == 13) WAITV(16); else if (ck == 14) WAITV(8); else WAITV(0);
    {
      bf16x8 (&o)[2][2] = ob[ck % K2_NBUF];
      asm volatile("" : "+v"(o[0][0]), "+v"(o[0][1]), "+v"(o[1][0]), "+v"(o[1][1]));
    }
    const unsigned sb = w * K2_WAVE_LDS + (ck % K2_NBUF) * 4096;
    bf16x8 fa[2][2];
#pragma unroll
    for (int ks = 0; ks < 2; ++ks)
#pragma unroll
      for (int nt = 0; nt < 2; ++nt) {
        const int row0 = 16 * ks + 8 * hi + q4, row1 = row0 + 4;
        const int ch = 4 * nt + 2 * blk + (pp >> 1);
        fa[ks][nt] = tr_read2(smem, sb + row0 * 128 + ((ch ^ (((row0 >> 1) & 1) << 2)) << 4) + 8 * (pp & 1),
                              sb + row1 * 128 + ((ch ^ (((row1 >> 1) & 1) << 2)) << 4) + 8 * (pp & 1));
      }
#pragma unroll
    for (int ks = 0; ks < 2; ++ks)
#pragma unroll
      for (int nt = 0; nt < 2; ++nt)
#pragma unroll
        for (int mt = 0; mt < 2; ++mt) acc[nt][mt] = mfma32(fa[ks][nt], ob[ck % K2_NBUF][mt][ks], acc[nt][mt]);
    asm volatile("s_waitcnt lgkmcnt(0)" ::: "memory");
  }
  __syncthreads();
  const int r32 = lane & 31;
#pragma unroll
  for (int nt = 0; nt < 2; ++nt)
#pragma unroll
    for (int mt = 0; mt < 2; ++mt)
#pragma unroll
      for (int qd = 0; qd < 4; ++qd) {
        f32x4 v = {acc[nt][mt][4 * qd], acc[nt][mt][4 * qd + 1], acc[nt][mt][4 * qd + 2], acc[nt][mt][4 * qd + 3]};
        *reinterpret_cast<f32x4*>(smem + ((size_t)w * 64 + 32 * mt + r32) * 272 + (32 * nt + 8 * qd + 4 * hi) * 4) = v;
      }
  __syncthreads();
  {
    const int m = tid >> 3, nc = tid & 7;
    float o[8];
#pragma unroll
    for (int j = 0; j < 8; ++j) o[j] = 0.f;
#pragma unroll
    for (int ww = 0; ww < 8; ++ww) {
      const f32x4 a = *reinterpret_cast<const f32x4*>(smem + ((size_t)ww * 64 + m) * 272 + nc * 32);
      const f32x4 c = *reinterpret_cast<const f32x4*>(smem + ((size_t)ww * 64 + m) * 272 + nc * 32 + 16);
#pragma unroll
      for (int j = 0; j < 4; ++j) { o[j] += a[j]; o[4 + j] += c[j]; }
    }
    const int mt = 2 * mg + (m >> 5), r = m & 31;
    int row;
    if (mt < 32) row = (mt >> 2) * SQ + (mt & 3) * 32 + r;
    else row = (2 * (mt - 32) + (r >> 4)) * SQ + 128 + (r & 15);
    bf16x8 v = pack8(o[0], o[1], o[2], o[3], o[4], o[5], o[6], o[7]);
    *reinterpret_cast<bf16x8*>((char*)out + ((size_t)row * (2 * DM) + p * DM + ng * 64 + nc * 8) * 2) = v;
  }
}

extern "C" void kernel_launch(void* const* d_in, const int* in_sizes, int n_in, void* d_out, int out_size, void* d_ws,
                              size_t ws_size, hipStream_t stream) {
  const __hip_bfloat16* xA = (const __hip_bfloat16*)d_in[0];
  const __hip_bfloat16* kA = (const __hip_bfloat16*)d_in[1];
  const __hip_bfloat16* vA = (const __hip_bfloat16*)d_in[2];
  const __hip_bfloat16* wqA = (const __hip_bfloat16*)d_in[3];
  const __hip_bfloat16* woA = (const __hip_bfloat16*)d_in[4];
  const __hip_bfloat16* xB = (const __hip_bfloat16*)d_in[5];
  const __hip_bfloat16* kB = (const __hip_bfloat16*)d_in[6];
  const __hip_bfloat16* vB = (const __hip_bfloat16*)d_in[7];
  const __hip_bfloat16* wqB = (const __hip_bfloat16*)d_in[8];
  const __hip_bfloat16* woB = (const __hip_bfloat16*)d_in[9];
  __hip_bfloat16* Of = (__hip_bfloat16*)d_ws;
  hipFuncSetAttribute((const void*)attn_kernel, hipFuncAttributeMaxDynamicSharedMemorySize, LDS_BYTES);
  hipFuncSetAttribute((const void*)oproj_kernel, hipFuncAttributeMaxDynamicSharedMemorySize, 8 * K2_WAVE_LDS);
  attn_kernel<<<256, 256, LDS_BYTES, stream>>>(xA, kA, vA, wqA, xB, kB, vB, wqB, Of, woA, woB
  );
  oproj_kernel<<<256, 512, 8 * K2_WAVE_LDS, stream>>>(Of, woA, woB, (__hip_bfloat16*)d_out);
}
```
